```python
import torch

def _softmax(x, axis=-1):
    return torch.softmax(x, dim=axis)
def _gelu(x):
    return torch.nn.functional.gelu(x, approximate="tanh")
def _silu(x):
    return torch.nn.functional.silu(x)
def _sigmoid(x):
    return torch.sigmoid(x)
def _layernorm(x, axis=-1, eps=1e-5):
    m = x.mean(dim=axis, keepdim=True)
    v = x.var(dim=axis, keepdim=True, unbiased=False)
    return (x - m) / torch.sqrt(v + eps)
def _scatter_add(tgt, idx, src, axis=0):
    return tgt.clone().index_add_(axis, idx, src)
def _segment_sum(src, seg, n):
    out = torch.zeros((n,) + src.shape[1:], dtype=src.dtype)
    return out.index_add_(0, seg, src)
def _pad(x, pw):
    flat = tuple(v for pair in reversed(pw) for v in pair)
    return torch.nn.functional.pad(x, flat)

kernel_name = 'recurrent_scan__linear_kernel_hedgehog__mixer_s_mlp__prompt'

def setup_inputs():

    _w = (lambda a: (lambda t: t.to(torch.bfloat16) if t.is_floating_point() else t)(torch.as_tensor(a)))
    def _A_setup():
        B, S, D, H = 1, 2048, 448, 16
        Dh = D // H
        return {
            "x": _w((lambda *s: torch.randn(*s, dtype=torch.bfloat16) * 0.05)(B, S, D)),
            "wqkv": _w((lambda *s: torch.randn(*s, dtype=torch.bfloat16) * 0.2)(D, 3 * D)),
            "w_phi": _w((lambda *s: torch.randn(*s, dtype=torch.bfloat16) * 0.2)(H, Dh, Dh) * (Dh ** -0.5)),
            "wo": _w((lambda *s: torch.randn(*s, dtype=torch.bfloat16) * 0.05)(D, D)),
        }
    _A_in = _A_setup()

    def _B_setup():
        B, S, D, F = 1, 2048, 448, 1792
        return {
            "x":  _w((lambda *s: torch.randn(*s, dtype=torch.bfloat16) * 0.05)(B, S, D)),
            "w1": _w((lambda *s: torch.randn(*s, dtype=torch.bfloat16) * 0.8)(D, D) * (D ** -0.5)),
            "w2": _w((lambda *s: torch.randn(*s, dtype=torch.bfloat16) * 0.8)(D, D) * (D ** -0.5)),
            "w3": _w((lambda *s: torch.randn(*s, dtype=torch.bfloat16) * 0.8)(D, F) * (D ** -0.5)),
            "w4": _w((lambda *s: torch.randn(*s, dtype=torch.bfloat16) * 0.8)(F, D) * (F ** -0.5)),
        }
    _B_in = _B_setup()

    return {
        "A_x": _A_in["x"],
        "A_wqkv": _A_in["wqkv"],
        "A_w_phi": _A_in["w_phi"],
        "A_wo": _A_in["wo"],
        "B_x": _B_in["x"],
        "B_w1": _B_in["w1"],
        "B_w2": _B_in["w2"],
        "B_w3": _B_in["w3"],
        "B_w4": _B_in["w4"],
    }

def reference(A_x, A_wqkv, A_w_phi, A_wo, B_x, B_w1, B_w2, B_w3, B_w4):
    def _A_ref(x, wqkv, w_phi, wo):
        B, S, D = x.shape
        H = 16; Dh = D // H
        qkv = (lambda x, *d: x.permute(*d))((x @ wqkv).reshape(B, S, 3, H, Dh), 2, 0, 3, 1, 4)
        q, k, v = qkv[0], qkv[1], qkv[2]
        pq = torch.einsum("bhsd,hdf->bhsf", q, w_phi)
        pk = torch.einsum("bhsd,hdf->bhsf", k, w_phi)
        phi_q = torch.concatenate([_softmax(pq, axis=-1), _softmax(-pq, axis=-1)], axis=-1)
        phi_k = torch.concatenate([_softmax(pk, axis=-1), _softmax(-pk, axis=-1)], axis=-1)
        kv = torch.einsum("bhsf,bhsd->bhfd", phi_k, v)
        z  = torch.einsum("bhsf->bhf", phi_k)
        num = torch.einsum("bhqf,bhfd->bhqd", phi_q, kv)
        den = torch.einsum("bhqf,bhf->bhq", phi_q, z)[..., None] + 1e-6
        o = num / den
        return (lambda x, *d: x.permute(*d))(o, 0, 2, 1, 3).reshape(B, S, D) @ wo

    def _B_ref(x, w1, w2, w3, w4):
        B, S, D = x.shape
        h = _gelu(_layernorm(x, axis=-1) @ w1)
        c = D // 4
        g0 = _pad(h[:, :, 0*c:1*c], ((0, 0), (1, 0), (0, 0)))[:, :S, :]
        g1 = _pad(h[:, :, 1*c:2*c], ((0, 0), (0, 1), (0, 0)))[:, 1:, :]
        g2 = _pad(h[:, :, 2*c:3*c], ((0, 0), (2, 0), (0, 0)))[:, :S, :]
        g3 = _pad(h[:, :, 3*c:   ], ((0, 0), (0, 2), (0, 0)))[:, 2:, :]
        hs = torch.concatenate([g0, g1, g2, g3], axis=-1) @ w2
        x1 = x + hs
        h2 = _gelu(_layernorm(x1, axis=-1) @ w3) @ w4
        return x1 + h2

    x = A_x
    xn = _layernorm(x, axis=-1)
    s = _A_ref(xn, A_wqkv, A_w_phi, A_wo)
    g = _silu(_B_ref(xn, B_w1, B_w2, B_w3, B_w4))
    return x + s * g
```

```cpp
#include <hip/hip_runtime.h>
#include <hip/hip_bf16.h>
typedef unsigned short u16;
constexpr int S=2048, D=448, H=16, DH=28, F=1792;
__device__ __forceinline__ float bf2f(u16 x){ return __uint_as_float(((unsigned)x)<<16); }
__device__ __forceinline__ u16 f2bf(float f){ unsigned u=__float_as_uint(f); u += 0x7fffu + ((u>>16)&1u); return (u16)(u>>16); }
__device__ __forceinline__ float rbf(float f){ return bf2f(f2bf(f)); }
__device__ __forceinline__ float gelu(float x){ return 0.5f*x*(1.f+tanhf(0.7978845608f*(x+0.044715f*x*x*x))); }

__global__ void ln_bf(const u16* in, float* out){ int r=blockIdx.x; __shared__ float s[64];
  float sum=0; for(int i=threadIdx.x;i<D;i+=64) sum+=bf2f(in[r*D+i]);
  for(int o=32;o;o>>=1) sum+=__shfl_xor(sum,o); float m=sum/D; float v=0;
  for(int i=threadIdx.x;i<D;i+=64){float d=bf2f(in[r*D+i])-m; v+=d*d;}
  for(int o=32;o;o>>=1) v+=__shfl_xor(v,o); v/=D; float rs=rsqrtf(v+1e-5f);
  for(int i=threadIdx.x;i<D;i+=64) out[r*D+i]=rbf((bf2f(in[r*D+i])-m)*rs);
}
__global__ void ln_f(const float* in, float* out){ int r=blockIdx.x;
  float sum=0; for(int i=threadIdx.x;i<D;i+=64) sum+=in[r*D+i];
  for(int o=32;o;o>>=1) sum+=__shfl_xor(sum,o); float m=sum/D; float v=0;
  for(int i=threadIdx.x;i<D;i+=64){float d=in[r*D+i]-m; v+=d*d;}
  for(int o=32;o;o>>=1) v+=__shfl_xor(v,o); v/=D; float rs=rsqrtf(v+1e-5f);
  for(int i=threadIdx.x;i<D;i+=64) out[r*D+i]=rbf((in[r*D+i]-m)*rs);
}
__global__ void gemm(const float* A, const u16* B, float* C, int N, int K, int act){
  int n=blockIdx.x*64+threadIdx.x; int m=blockIdx.y; if(n>=N) return; float acc=0;
  for(int k=0;k<K;k++) acc+=A[m*K+k]*bf2f(B[k*N+n]);
  acc=rbf(acc); if(act==1) acc=rbf(gelu(acc)); C[m*N+n]=acc;
}
__global__ void phi(const float* qkv, const u16* wphi, float* pq, float* pk){
  int idx=blockIdx.x*64+threadIdx.x; if(idx>=S*H*2) return; int which=idx/(S*H); int s=(idx%(S*H))/H, h=idx%H;
  float p[DH]; for(int f=0;f<DH;f++){ float a=0; for(int d=0;d<DH;d++) a+=qkv[s*3*D+which*D+h*DH+d]*bf2f(wphi[h*DH*DH+d*DH+f]); p[f]=rbf(a);}
  float mx=-1e30f,mn=1e30f; for(int f=0;f<DH;f++){mx=fmaxf(mx,p[f]);mn=fminf(mn,p[f]);}
  float s1=0,s2=0; for(int f=0;f<DH;f++){s1+=expf(p[f]-mx); s2+=expf(mn-p[f]);}
  float* o=(which?pk:pq)+(s*H+h)*56; for(int f=0;f<DH;f++){o[f]=rbf(expf(p[f]-mx)/s1); o[28+f]=rbf(expf(mn-p[f])/s2);}
}
__global__ void kvk(const float* qkv, const float* pk, float* kv){
  int idx=blockIdx.x*64+threadIdx.x; if(idx>=H*56*29) return; int h=idx/(56*29), f=(idx/29)%56, d=idx%29; float a=0;
  for(int s=0;s<S;s++) a+=pk[(s*H+h)*56+f]*(d<28?qkv[s*3*D+2*D+h*DH+d]:1.f); kv[idx]=rbf(a);
}
__global__ void ok(const float* pq, const float* kv, float* o){ int idx=blockIdx.x*64+threadIdx.x; if(idx>=S*D) return; int s=idx/D, c=idx%D, h=c/DH, d=c%DH;
  float n=0,de=0; for(int f=0;f<56;f++){ float p=pq[(s*H+h)*56+f]; n+=p*kv[(h*56+f)*29+d]; de+=p*kv[(h*56+f)*29+28]; }
  o[idx]=rbf(rbf(n)/rbf(rbf(de)+1e-6f));
}
__global__ void shiftk(const float* h, float* g){ int idx=blockIdx.x*64+threadIdx.x; if(idx>=S*D) return; int s=idx/D,c=idx%D; int grp=c/112; int src= grp==0? s-1: grp==1? s+1: grp==2? s-2: s+2; g[idx]=(src>=0&&src<S)?h[src*D+c]:0.f; }
__global__ void addk(const float* a,const float* b,float* c){ int idx=blockIdx.x*64+threadIdx.x; if(idx>=S*D) return; c[idx]=rbf(a[idx]+b[idx]); }
__global__ void fin(const u16* x,const float* sv,const float* x1,const float* h2,u16* out){ int idx=blockIdx.x*64+threadIdx.x; if(idx>=S*D) return; float b=rbf(x1[idx]+h2[idx]); float g=rbf(b/(1.f+expf(-b))); out[idx]=f2bf(bf2f(x[idx])+rbf(sv[idx]*g)); }

extern "C" void kernel_launch(void* const* d_in, const int* in_sizes, int n_in, void* d_out, int out_size, void* d_ws, size_t ws_size, hipStream_t stream){
  const u16* x=(const u16*)d_in[0]; const u16* wqkv=(const u16*)d_in[1]; const u16* wphi=(const u16*)d_in[2]; const u16* wo=(const u16*)d_in[3];
  const u16* w1=(const u16*)d_in[5]; const u16* w2=(const u16*)d_in[6]; const u16* w3=(const u16*)d_in[7]; const u16* w4=(const u16*)d_in[8];
  float* ws=(float*)d_ws; float* xn=ws; float* xnn=xn+S*D; float* qkv=xnn+S*D; float* pq=qkv+S*3*D; float* pk=pq+S*H*56; float* kv=pk+S*H*56; float* o=kv+65536; float* sv=o+S*D;
  float* h=sv+S*D; float* g=h+S*D; float* hs=g+S*D; float* x1=hs+S*D; float* x1n=x1+S*D; float* h3=x1n+S*D; float* h2=h3+S*F;
  int E=(S*D+63)/64;
  ln_bf<<<S,64,0,stream>>>(x,xn); ln_f<<<S,64,0,stream>>>(xn,xnn);
  gemm<<<dim3(21,S),64,0,stream>>>(xn,wqkv,qkv,3*D,D,0);
  phi<<<(S*H*2+63)/64,64,0,stream>>>(qkv,wphi,pq,pk);
  kvk<<<(H*56*29+63)/64,64,0,stream>>>(qkv,pk,kv);
  ok<<<E,64,0,stream>>>(pq,kv,o);
  gemm<<<dim3(7,S),64,0,stream>>>(o,wo,sv,D,D,0);
  gemm<<<dim3(7,S),64,0,stream>>>(xnn,w1,h,D,D,1);
  shiftk<<<E,64,0,stream>>>(h,g);
  gemm<<<dim3(7,S),64,0,stream>>>(g,w2,hs,D,D,0);
  addk<<<E,64,0,stream>>>(xn,hs,x1);
  ln_f<<<S,64,0,stream>>>(x1,x1n);
  gemm<<<dim3(28,S),64,0,stream>>>(x1n,w3,h3,F,D,1);
  gemm<<<dim3(7,S),64,0,stream>>>(h3,w4,h2,D,F,0);
  fin<<<E,64,0,stream>>>(x,sv,x1,h2,(u16*)d_out);
}
```

```cpp
#include <hip/hip_runtime.h>
#include <hip/hip_bf16.h>
#include <stdint.h>

#define GAS __attribute__((address_space(1)))
#define LAS __attribute__((address_space(3)))
#define DI __device__ __forceinline__
#define RLX_AGENT __ATOMIC_RELAXED, __HIP_MEMORY_SCOPE_AGENT
typedef unsigned long long u64;
typedef unsigned v4u __attribute__((ext_vector_type(4)));
typedef unsigned v2u __attribute__((ext_vector_type(2)));
typedef float v4f __attribute__((ext_vector_type(4)));
typedef float f2 __attribute__((ext_vector_type(2)));
typedef __bf16 bf2 __attribute__((ext_vector_type(2)));
typedef short s16x4 __attribute__((ext_vector_type(4)));
typedef short bf16x8 __attribute__((ext_vector_type(8)));
typedef float f32x16 __attribute__((ext_vector_type(16)));
typedef __amdgpu_buffer_rsrc_t rsrc_t;
typedef LAS unsigned char* lds_t;

#define FSTR 256
constexpr unsigned WS_FLAG = 4096, FLAG_STRIDE = FSTR;
constexpr unsigned FH = 0, FX = 896, FH2 = 1792, FKVP = 2688, FKVT = 3200, FO = 3712, NFLAG = 3840;
constexpr unsigned H_OFF = 1u << 20;
constexpr unsigned X1_OFF = H_OFF + 64 * 28 * 1024;
constexpr unsigned H2_OFF = X1_OFF + 64 * 28 * 1024;
constexpr unsigned O_OFF = H2_OFF + 64 * 112 * 1024;
constexpr unsigned KVP_OFF = O_OFF + 64 * 32 * 1024;
constexpr unsigned KVT_OFF = KVP_OFF + 64 * 32 * 1024;
constexpr unsigned XS_OFF = KVT_OFF + 64 * 1024;
constexpr unsigned WS_USED = XS_OFF + 64 * 14 * 256;
constexpr unsigned DBG_OFF = 32u << 20;
static_assert(WS_FLAG + NFLAG * FLAG_STRIDE <= H_OFF, "flags");

constexpr unsigned SLOT = 8192, MISC = 19 * SLOT, LDS_BYTES = 163840;
constexpr unsigned M_AL = MISC + 0, M_AR = MISC + 16, M_BL = MISC + 32, M_BR = MISC + 48, M_BX = MISC + 64, M_ACS = MISC + 80, M_BS = MISC + 96, M_BC = MISC + 112, M_OR = MISC + 136, M_TAG = MISC + 128;
constexpr unsigned M_MU = MISC + 256, M_R = MISC + 512, M_IR = MISC + 768;
constexpr unsigned M_CS1 = MISC + 1024;
constexpr unsigned M_CSK = MISC + 1536;
constexpr unsigned M_CS3 = MISC + 2048;
constexpr unsigned M_PHI = MISC + 2560;
constexpr unsigned M_ST = M_CS3;
constexpr unsigned M_CSP0 = M_CS1, M_CSP2 = M_PHI + 5120;
static_assert(M_CSP2 + 512 <= LDS_BYTES, "misc");

#define PRESLEEP 8
#define PRE2 5
#define PRE3 2
#define PRE1 0
#define LGAP 3
#define WSLP 1
#define WPACE() asm volatile("s_sleep %0" ::"i"(WSLP) : "memory")
#define XLAG 6
#define WARM_AT 3
#define W3AT 1
#define XROT (c == 7 ? rb % 7 : 0)
#define OAT 10
#define VPRIO 0
#define KPRIO 0
constexpr float LN_EPS = 1e-5f;
constexpr float LOG2E = 1.4426950408889634f;
constexpr unsigned SPIN_CAP = 1u << 15;

#define STAMP(i)
#define STAMP_DECL
#define STAMP_OUT()

#define INL __attribute__((always_inline))
template <int V> struct IC { static constexpr int value = V; };
template <int I, int N, class F> DI void sfor(F&& f) { if constexpr (I < N) { f(IC<I>{}); sfor<I + 1, N>(f); } }
#define SCHED() __builtin_amdgcn_sched_barrier(0)
DI unsigned rfl(unsigned x) { return __builtin_amdgcn_readfirstlane(x); }
DI float u2f(unsigned x) { return __builtin_bit_cast(float, x); }
DI unsigned f2u(float x) { return __builtin_bit_cast(unsigned, x); }
DI unsigned pk2(float a, float b) { f2 v = {a, b}; bf2 r = __builtin_convertvector(v, bf2); return __builtin_bit_cast(unsigned, r); }
DI float bflo(unsigned x) { return u2f(x << 16); }
DI float bfhi(unsigned x) { return u2f(x & 0xffff0000u); }
DI float dot2(unsigned a, unsigned b, float c) { return __builtin_amdgcn_fdot2_f32_bf16(__builtin_bit_cast(bf2, a), __builtin_bit_cast(bf2, b), c, false); }
DI float sum8(v4u a, float c) { const unsigned x = a.x, y = a.y, z = a.z, w = a.w; return dot2(w, 0x3f803f80u, dot2(z, 0x3f803f80u, dot2(y, 0x3f803f80u, dot2(x, 0x3f803f80u, c)))); }
DI float sq8(v4u a, float c) { const unsigned x = a.x, y = a.y, z = a.z, w = a.w; return dot2(w, w, dot2(z, z, dot2(y, y, dot2(x, x, c)))); }
DI void sum8x2_if(int w, int want, v4u a, v4u b, float& sa, float& sb) {
  const unsigned a0 = a.x, a1 = a.y, a2 = a.z, a3 = a.w, b0 = b.x, b1 = b.y, b2 = b.z, b3 = b.w;
  asm volatile("s_cmp_eq_u32 %2, %3\n\ts_cbranch_scc0 1f\n\t"
               "v_dot2c_f32_bf16 %0, 0x3f803f80, %4\n\tv_dot2c_f32_bf16 %1, 0x3f803f80, %8\n\tv_dot2c_f32_bf16 %0, 0x3f803f80, %5\n\tv_dot2c_f32_bf16 %1, 0x3f803f80, %9\n\t"
               "v_dot2c_f32_bf16 %0, 0x3f803f80, %6\n\tv_dot2c_f32_bf16 %1, 0x3f803f80, %10\n\tv_dot2c_f32_bf16 %0, 0x3f803f80, %7\n\tv_dot2c_f32_bf16 %1, 0x3f803f80, %11\n1:"
               : "+v"(sa), "+v"(sb) : "s"(w), "s"(want), "v"(a0), "v"(a1), "v"(a2), "v"(a3), "v"(b0), "v"(b1), "v"(b2), "v"(b3) : "scc");
}
DI f32x16 mfma(v4u a, v4u b, f32x16 c) { return __builtin_amdgcn_mfma_f32_32x32x16_bf16(__builtin_bit_cast(bf16x8, a), __builtin_bit_cast(bf16x8, b), c, 0, 0, 0); }
DI float other_half(float x) {
  const unsigned u = f2u(x);
  const auto r = __builtin_amdgcn_permlane32_swap(u, u, false, false);
  const unsigned a = r[0], b = r[1];
  return u2f(a ^ b ^ u);
}
#define HALF_PAIR(a, b, lo, hi) do { const auto r_ = __builtin_amdgcn_permlane32_swap(f2u(a), f2u(b), false, false); const unsigned l_ = r_[0], h_ = r_[1]; lo = u2f(l_); hi = u2f(h_); } while (0)
DI v4u pack8(const f32x16& a, int s) { v4u r; r.x = pk2(a[8 * s], a[8 * s + 1]); r.y = pk2(a[8 * s + 2], a[8 * s + 3]); r.z = pk2(a[8 * s + 4], a[8 * s + 5]); r.w = pk2(a[8 * s + 6], a[8 * s + 7]); return r; }
struct GeluK { float k1, k2, ir; };
DI GeluK gelu_k(float r, float ir) { return GeluK{-2.30220820f * r, -0.10294324f * r * r * r, ir}; }
DI float gelu(float a, const GeluK& k) {
  const float e = __builtin_amdgcn_exp2f(a * __builtin_fmaf(a * a, k.k2, k.k1));
  return a * __builtin_amdgcn_rcpf(__builtin_fmaf(e, k.ir, k.ir));
}
DI v4u lds128(lds_t L, unsigned a) { return *(const LAS v4u*)(L + a); }
DI v4f ldsf4(lds_t L, unsigned a) { return *(const LAS v4f*)(L + a); }
DI v4u wfrag(lds_t L, unsigned a0, unsigned a1) {
  const s16x4 lo = __builtin_amdgcn_ds_read_tr16_b64_v4i16((LAS s16x4*)(L + a0));
  const s16x4 hi = __builtin_amdgcn_ds_read_tr16_b64_v4i16((LAS s16x4*)(L + a1));
  return __builtin_bit_cast(v4u, __builtin_shufflevector(lo, hi, 0, 1, 2, 3, 4, 5, 6, 7));
}
DI unsigned opq(unsigned x) { asm("" : "+v"(x)); return x; }
#define PIN(x) asm volatile("" : "+v"(x))
template <int N> DI void waitvm() { asm volatile("s_waitcnt vmcnt(%0)" ::"i"(N) : "memory"); }
DI void waitlgkm() { asm volatile("s_waitcnt lgkmcnt(0)" ::: "memory"); }
DI void bar() { asm volatile("s_waitcnt lgkmcnt(0)\n\ts_barrier" ::: "memory"); }
DI void dma(unsigned ldsa, unsigned voff, rsrc_t rs, unsigned soff) {
  asm volatile("s_mov_b32 m0, %0\n\ts_nop 0\n\tbuffer_load_dwordx4 %1, %2, %3 offen lds" ::"s"(ldsa), "v"(voff), "s"(rs), "s"(soff) : "memory");
}
template <int N, int STEP> DI void dman(unsigned ldsa, unsigned voff, rsrc_t rs, unsigned soff) {
  unsigned t;
#define DMA_NEXT "\n\ts_add_u32 m0, m0, 0x400\n\ts_add_u32 %0, %0, %5\n\tbuffer_load_dwordx4 %2, %3, %0 offen lds"
#define DMA_FIRST "s_mov_b32 m0, %1\n\ts_mov_b32 %0, %4\n\tbuffer_load_dwordx4 %2, %3, %0 offen lds"
  if (N == 2) asm volatile(DMA_FIRST DMA_NEXT : "=&s"(t) : "s"(ldsa), "v"(voff), "s"(rs), "s"(soff), "i"(STEP) : "memory", "scc");
  if (N == 3) asm volatile(DMA_FIRST DMA_NEXT DMA_NEXT : "=&s"(t) : "s"(ldsa), "v"(voff), "s"(rs), "s"(soff), "i"(STEP) : "memory", "scc");
  if (N == 4) asm volatile(DMA_FIRST DMA_NEXT DMA_NEXT DMA_NEXT : "=&s"(t) : "s"(ldsa), "v"(voff), "s"(rs), "s"(soff), "i"(STEP) : "memory", "scc");
  if (N == 8) asm volatile(DMA_FIRST DMA_NEXT DMA_NEXT DMA_NEXT DMA_NEXT DMA_NEXT DMA_NEXT DMA_NEXT : "=&s"(t) : "s"(ldsa), "v"(voff), "s"(rs), "s"(soff), "i"(STEP) : "memory", "scc");
}
DI v4u wld(rsrc_t rs, unsigned voff, unsigned soff) { return __builtin_amdgcn_raw_buffer_load_b128(rs, voff, soff, 0); }
DI v4u gld(rsrc_t rs, unsigned voff, unsigned soff) { return __builtin_amdgcn_raw_buffer_load_b128(rs, voff, soff, 16); }
DI void gst(v4u v, rsrc_t rs, unsigned voff, unsigned soff) { __builtin_amdgcn_raw_buffer_store_b128(v, rs, voff + soff, 0, 16); }
DI void lset(unsigned a, unsigned v) { asm volatile("ds_write_b32 %0, %1" ::"v"(a), "v"(v) : "memory"); }
template <unsigned OFF, int SLP> DI void lwait(unsigned a, unsigned v) {
  unsigned x, n;
  asm volatile("s_mov_b32 %1, %4\n"
               "1:\n\tds_read_b32 %0, %2 offset:%5\n\ts_waitcnt lgkmcnt(0)\n\tv_cmp_gt_u32 vcc, %3, %0\n\ts_cbranch_vccz 2f\n\t"
               "s_sleep %6\n\ts_sub_u32 %1, %1, 1\n\ts_cbranch_scc0 1b\n2:"
               : "=&v"(x), "=&s"(n) : "v"(a), "s"(v), "i"(SPIN_CAP), "i"(OFF), "i"(SLP) : "memory", "scc", "vcc");
}
template <unsigned OFF> DI unsigned lpeek(unsigned a) {
  unsigned x;
  asm volatile("ds_read_b32 %0, %1 offset:%2" : "=v"(x) : "v"(a), "i"(OFF) : "memory");
  return x;
}
template <int N> DI u64 lcmp(unsigned x, unsigned v) {
  u64 m;
  asm volatile("s_waitcnt lgkmcnt(%3)\n\tv_cmp_gt_u32 %0, %2, %1" : "=s"(m) : "v"(x), "s"(v), "i"(N) : "memory");
  return m;
}
template <unsigned OFF> DI void lbranch(u64 m, unsigned a, unsigned v) {
  unsigned x, n;
  asm volatile("s_cmp_eq_u64 %2, 0\n\ts_cbranch_scc1 2f\n\ts_mov_b32 %1, %5\n"
               "3:\n\tds_read_b32 %0, %3 offset:%6\n\ts_waitcnt lgkmcnt(0)\n\tv_cmp_gt_u32 vcc, %4, %0\n\ts_cbranch_vccz 2f\n\t"
               "s_sub_u32 %1, %1, 1\n\ts_cbranch_scc0 3b\n2:"
               : "=&v"(x), "=&s"(n) : "s"(m), "v"(a), "s"(v), "i"(SPIN_CAP), "i"(OFF) : "memory", "scc", "vcc");
}
#define lpeek4(word) lpeek<(word) - MISC>(m4)
#define lbranch4(m, word, v) lbranch<(word) - MISC>(m, m4, v)
#define MM(acc, a, b) do { acc = mfma(a, b, acc); PIN(acc); SCHED(); } while (0)
#define lwait4(word, v) lwait<(word) - MISC, 1>(m4, v)
#define lwait4f(word, v) lwait<(word) - MISC, 0>(m4, v)
DI void publish(unsigned char* ws, unsigned idx, u64 epoch) {
  const unsigned off = WS_FLAG + idx * FLAG_STRIDE;
  asm volatile("s_waitcnt vmcnt(0)\n\ts_mov_b64 exec, 1\n\tglobal_store_dwordx2 %0, %1, %2 sc1\n\ts_mov_b64 exec, -1" ::"v"(off), "v"(epoch), "s"(ws) : "memory");
}
DI void publish8(unsigned char* ws, unsigned off, u64 epoch) {
  asm volatile("s_waitcnt vmcnt(0)\n\ts_mov_b64 exec, 0xff\n\tglobal_store_dwordx2 %0, %1, %2 sc1\n\ts_mov_b64 exec, -1" ::"v"(off), "v"(epoch), "s"(ws) : "memory");
}
DI unsigned flag_at(unsigned idx) { return WS_FLAG + idx * FLAG_STRIDE; }
template <int N, int PRE, int GAP> DI void poll(unsigned char* ws, unsigned off, u64 epoch) {
  u64 x, z = 0; unsigned n;
  const u64 mask = N == 64 ? ~0ull : (1ull << (N & 63)) - 1;
  asm volatile("s_mov_b32 %1, %6\n\ts_mov_b64 exec, %7\n\ts_sleep %8\n"
               "1:\n\tglobal_atomic_add_x2 %0, %2, %5, %3 sc0 sc1\n\ts_waitcnt vmcnt(0)\n\tv_cmp_ne_u64 vcc, %4, %0\n\ts_cbranch_vccz 2f\n\t"
               "s_sleep %9\n\ts_sub_u32 %1, %1, 1\n\ts_cbranch_scc0 1b\n2:\n\ts_mov_b64 exec, -1"
               : "=&v"(x), "=&s"(n) : "v"(off), "s"(ws), "v"(epoch), "v"(z), "i"(SPIN_CAP), "s"(mask), "i"(PRE), "i"(GAP) : "memory", "scc", "vcc");
}

DI void dual_softmax(const f32x16& p, float sc, float post, int hh, v4u (&phi)[4]) {
  float mx = fmaxf(fmaxf(p[0], p[1]), p[2]), mn = fminf(fminf(p[0], p[1]), p[2]);
#pragma unroll
  for (int r = 3; r < 15; r += 2) { mx = fmaxf(fmaxf(mx, p[r]), p[r + 1]); mn = fminf(fminf(mn, p[r]), p[r + 1]); }
  mx = fmaxf(mx, p[15]); mn = -fminf(mn, p[15]);
  float a, b;
  HALF_PAIR(mx, mn, a, b);
  const float t = -fmaxf(a, b) * sc;
  float bx, bn;
  HALF_PAIR(t, t, bx, bn);
  f32x16 e1, e2;
#pragma unroll
  for (int r = 0; r < 16; ++r) {
    e1[r] = __builtin_amdgcn_exp2f(__builtin_fmaf(p[r], sc, bx));
    e2[r] = __builtin_amdgcn_exp2f(__builtin_fmaf(p[r], -sc, bn));
  }
  float s1 = (e1[12] + e1[13]) + (e1[14] + e1[15]), s2 = (e2[12] + e2[13]) + (e2[14] + e2[15]);
  s1 = hh ? 0.f : s1; s2 = hh ? 0.f : s2;
#pragma unroll
  for (int r = 0; r < 12; ++r) { s1 += e1[r]; s2 += e2[r]; }
  HALF_PAIR(s1, s2, a, b);
  const float iv = __builtin_amdgcn_rcpf(a + b) * post;
  float i1, i2;
  HALF_PAIR(iv, iv, i1, i2);
#pragma unroll
  for (int r = 0; r < 16; ++r) { e1[r] *= i1; e2[r] *= i2; }
  phi[0] = pack8(e1, 0); phi[1] = pack8(e1, 1); phi[2] = pack8(e2, 0); phi[3] = pack8(e2, 1);
}
DI void fold_mean(lds_t L, f32x16& a, float mu, unsigned cs_addr, int hh) {
#pragma unroll
  for (int g = 0; g < 4; ++g) {
    const v4f cs = ldsf4(L, cs_addr + g * 32 + hh * 16);
#pragma unroll
    for (int j = 0; j < 4; ++j) a[4 * g + j] = __builtin_fmaf(-mu, cs[j], a[4 * g + j]);
  }
}
DI void feature_map(v4u k0, v4u k1, float r, float post, const v4u (&wp)[2], int hh, v4u (&phi)[4]) {
  f32x16 p = {};
  p = mfma(wp[0], k0, p);
  p = mfma(wp[1], k1, p);
  dual_softmax(p, r * LOG2E, post, hh, phi);
}
DI v4u ident(int s, int lane) {
  const int hh = lane >> 5, l31 = lane & 31;
  unsigned w_[4];
#pragma unroll
  for (int d = 0; d < 4; ++d) {
    const int k0 = 16 * s + 8 * (d >> 1) + 4 * hh + 2 * (d & 1);
    w_[d] = (l31 == k0 ? 0x3f80u : 0u) | (l31 == k0 + 1 ? 0x3f800000u : 0u);
  }
  return v4u{w_[0], w_[1], w_[2], w_[3]};
}
DI void load_wphi(lds_t L, unsigned base, int lane, v4u (&wp)[2]) {
  const int hh = lane >> 5, q = (lane & 15) >> 2;
  const unsigned a = base + (4 * hh + q) * 56 + min(32 * ((lane >> 4) & 1) + 8 * (lane & 3), 48);
  wp[0] = wfrag(L, a, a + 8 * 56);
  wp[1] = wfrag(L, a + 16 * 56, a + (hh ? 20 : 24) * 56);
  wp[1].z = hh ? 0u : wp[1].z;
  wp[1].w = hh ? 0u : wp[1].w;
}

constexpr int KSLOT[14] = {14, 15, 16, 17, 18, 7, 8, 9, 10, 11, 12, 13, 14, 15};
constexpr int BSLOT[7] = {16, 17, 18, 7, 8, 9, 10};
constexpr int VSLOT[14] = {11, 12, 13, 14, 15, 18, 7, 8, 9, 10, 11, 12, 13, 14};
constexpr int CSLOT[14] = {16, 17, 15, 18, 7, 8, 9, 0, 1, 2, 3, 4, 5, 6};
constexpr int DRING[12] = {10, 11, 12, 13, 14, 16, 17, 15, 18, 7, 8, 9};
constexpr unsigned XCH = 16 * SLOT, XCH4 = 15 * SLOT;
DI constexpr int d_al(int u) { return u < 2 ? 26 : u < 6 ? 27 : 23 + u; }

#if defined(__HIP_DEVICE_COMPILE__)
__attribute__((target("no-packed-fp32-ops")))
#endif
__global__ void __launch_bounds__(512, 2)
fused_kernel(const unsigned short* x_, const unsigned short* wqkv_, const unsigned short* wphi_, const unsigned short* wo_, const unsigned short* w1_,
             const unsigned short* w2_, const unsigned short* w3_, const unsigned short* w4_, unsigned short* out_, unsigned char* ws_) {
  STAMP_DECL;
  STAMP(0);
  typedef u64 u64x8 __attribute__((ext_vector_type(8)));
  typedef u64 u64x2 __attribute__((ext_vector_type(2)));
  u64x8 ka; u64x2 kb;
  asm volatile("s_load_dwordx16 %0, %2, 0x0\n\ts_load_dwordx4 %1, %2, 0x40" : "=&s"(ka), "=&s"(kb) : "s"(__builtin_amdgcn_kernarg_segment_ptr()) : "memory");
  extern __shared__ __attribute__((aligned(16))) unsigned char smem[];
  const lds_t L = (lds_t)smem;
  const int tid = threadIdx.x, lane = tid & 63, wv = __builtin_amdgcn_readfirstlane(tid >> 6);
  const int b = blockIdx.x, rb = 4 * (b & 7) + (b >> 6), c = (b >> 3) & 7;
  const int hh = lane >> 5, l31 = lane & 31, cb = (lane >> 4) & 1, q4 = (lane & 15) >> 2, p4 = lane & 3;
  const int aw = wv & 3;
  const unsigned dm = c == 7 ? 2048 : 0;
  if (tid < 40) *(LAS unsigned*)(L + MISC + dm + 4 * tid) = 0u;
  const unsigned wsw = 16 * ((lane & 7) ^ (((lane >> 4) & 1) << 2));
  unsigned vW896 = (lane >> 3) * 896 + wsw;
  unsigned vX0 = (lane >> 3) * 896 + 16 * ((lane & 7) ^ (lane >> 4)), vX1 = vX0 ^ 64;
  PIN(vW896); PIN(vX0); PIN(vX1);
  asm volatile("s_waitcnt lgkmcnt(0)\n\ts_barrier" : "+s"(ka), "+s"(kb) :: "memory");
  const u64 k0 = ka[0], k1 = ka[1], k2 = ka[2], k3 = ka[3], k4 = ka[4], k5 = ka[5], k6 = ka[6], k7 = ka[7], k8 = kb[0], k9 = kb[1];
  const unsigned short *x = (const unsigned short*)k0, *wqkv = (const unsigned short*)k1, *wphi = (const unsigned short*)k2, *wo = (const unsigned short*)k3;
  const unsigned short *w1 = (const unsigned short*)k4, *w2 = (const unsigned short*)k5, *w3 = (const unsigned short*)k6, *w4 = (const unsigned short*)k7;
  unsigned short* out = (unsigned short*)k8;
  unsigned char* ws = (unsigned char*)k9;
  u64 pc0;
  asm volatile("s_getpc_b64 %0" : "=s"(pc0));
#define READ_TAG() u64 epoch = __hip_atomic_load((GAS u64*)ws, RLX_AGENT)
#define TAKE_TAG() do { asm volatile("" : "+v"(epoch)); epoch += 1; *(LAS u64*)(L + M_TAG + dm) = epoch; } while (0)
  const rsrc_t rsX = __builtin_amdgcn_make_buffer_rsrc((void*)x, 0, 2048 * 896, 0x00020000);
  const rsrc_t rsQ = __builtin_amdgcn_make_buffer_rsrc((void*)wqkv, 0, 448 * 2688, 0x00020000);
  const rsrc_t rsP = __builtin_amdgcn_make_buffer_rsrc((void*)wphi, 0, 16 * 1568, 0x00020000);
  const rsrc_t rsW = __builtin_amdgcn_make_buffer_rsrc((void*)ws, 0, WS_USED, 0x00020000);
  const unsigned vW2688 = (lane >> 3) * 2688 + wsw, vW3584 = (lane >> 3) * 3584 + wsw;
  const unsigned rsw = ((q4 >> 1) & 1) << 2;
  const unsigned l16 = lane * 16;
  unsigned xa[4];
#pragma unroll
  for (int s = 0; s < 4; ++s) xa[s] = l31 * 128 + 16 * ((2 * s + hh) ^ ((l31 >> 1) & 7));
  const unsigned m4 = MISC + dm + 4 * (lane & 3);
  const unsigned al = M_AL + dm + 4 * aw, ar = M_AR + 4 * aw, br = M_BR + dm + 4 * aw;
  const int rho = XROT;
  auto rot = [&](int j) INL { return j + rho >= 7 ? j + rho - 7 : j + rho; };
  auto dma_x = [&](int j) INL {
    dma(j * SLOT + 2 * aw * 1024, vX0, rsX, (64 * rb + 16 * aw) * 896 + 128 * rot(j));
    dma(j * SLOT + (2 * aw + 1) * 1024, vX1, rsX, (64 * rb + 16 * aw + 8) * 896 + 128 * rot(j));
  };
  const rsrc_t rs1 = __builtin_amdgcn_make_buffer_rsrc((void*)w1, 0, 448 * 896, 0x00020000);
  auto dma_w1 = [&](int j) INL { dman<2, 8 * 896>((7 + j) * SLOT + aw * 2048, vW896, rs1, (64 * j + 16 * aw) * 896 + 128 * c); };
  if (c == 7) { if (wv >= 4) { dma_x(0); dma_x(1); } }
  else if (wv < 4) { dma_x(0); dma_w1(0); dma_x(1); }
  else { dma_w1(1); dma_x(2); }
  SCHED();

  if (c == 7) {
    constexpr unsigned RING = 7 * SLOT, RSLOT = 14336, NR = 7, QCS = MISC + 2048 + 256, QST = QCS + 2048, QRED = RING;
    static_assert(RING + NR * RSLOT == MISC + 2048 && QST + 512 <= LDS_BYTES, "Q map");
    if (wv >= 4) {
#pragma unroll
      for (int j = 2; j < 7; ++j) dma_x(j);
      const unsigned lo = RING + (aw >> 1) * 7168 + (aw & 1) * 4096, so = 8 * (aw >> 1) * 2688 + (aw & 1) * 512;
      unsigned ring = 0;
      auto phi_in = [&]() INL {
#pragma unroll
        for (int i = 0; i < 7; ++i) { const unsigned p = min(7 * aw + i, 24) * 1024; dma(p, l16 + p, rsP, 0); }
      };
      if (aw & 1) {
#pragma unroll 1
        for (int it = 0; it < 28; ++it) {
          if (it >= NR) lwait4f(M_BR, it - NR + 1);
          dman<3, 128>(lo + ring, vW2688, rsQ, so + (it + 4 * rho >= 28 ? it + 4 * rho - 28 : it + 4 * rho) * 16 * 2688);
          if (it >= 4) { waitvm<12>(); lset(al, it - 3); }
          ring = ring == (NR - 1) * RSLOT ? 0 : ring + RSLOT;
        }
        phi_in();
        waitvm<16>(); lset(al, 25); waitvm<13>(); lset(al, 26); waitvm<10>(); lset(al, 27);
      } else {
#pragma unroll 1
        for (int it = 0; it < 28; ++it) {
          if (it >= NR) lwait4f(M_BR, it - NR + 1);
          dman<4, 128>(lo + ring, vW2688, rsQ, so + (it + 4 * rho >= 28 ? it + 4 * rho - 28 : it + 4 * rho) * 16 * 2688);
          if (it >= 4) { waitvm<16>(); lset(al, it - 3); }
          ring = ring == (NR - 1) * RSLOT ? 0 : ring + RSLOT;
        }
        phi_in();
        waitvm<19>(); lset(al, 25); waitvm<15>(); lset(al, 26); waitvm<11>(); lset(al, 27);
      }
      waitvm<7>(); lset(al, 28);
      {
        const unsigned o = lane < 32 ? KVP_OFF + (2 * rb * 32 + lane * 2) * 1024 : lane < 40 ? KVT_OFF + (lane - 32) * 8192 : lane < 48 ? O_OFF + rb * 65536 + (lane - 40) * 8192 : WS_FLAG + (FKVP + (lane - 48) * 36) * FLAG_STRIDE;
        unsigned t_ = __builtin_amdgcn_raw_buffer_load_b32(rsW, o, 0, 16);
        asm volatile("" ::"v"(t_));
      }
      waitvm<0>();
      bar();
      const u64 epoch = *(const LAS u64*)(L + M_TAG + 2048);
      STAMP(2);
      __builtin_amdgcn_s_setprio(3);
      v4u id0 = ident(0, lane);
      PIN(id0);
      const int blk = 2 * rb + (aw >> 1), r0 = 16 * (aw & 1);
      poll<16, 0, LGAP>(ws, flag_at(FKVP + (r0 + (lane & 15)) * 16 + (blk >> 2)), epoch);
      STAMP(3);
      v4u pv[16];
#pragma unroll
      for (int i = 0; i < 16; ++i) pv[i] = gld(rsW, l16, KVP_OFF + (blk * 32 + r0 + i) * 1024);
      SCHED();
      f32x16 a = {};
#pragma unroll
      for (int i = 0; i < 16; ++i) a = mfma(id0, pv[i], a);
      PIN(a);
      STAMP(9);
      const unsigned qr = QRED + (aw >> 1) * 2048 + l16, qf = M_BX + 2048 + 4 * (aw >> 1);
      if (aw & 1) {
        *(LAS v4f*)(L + qr) = v4f{a[0], a[1], a[2], a[3]};
        *(LAS v4f*)(L + qr + 1024) = v4f{a[4], a[5], a[6], a[7]};
        lset(qf, 1);
        if (rb == 0 && aw == 1) {
          poll<64, 0, 16>(ws, flag_at(FKVT + lane), epoch);
          if (lane == 0) __hip_atomic_store((GAS u64*)ws, epoch, RLX_AGENT);
        }
      } else {
        lwait<0, 0>(qf, 1);
        STAMP(10);
        const v4f lo_ = ldsf4(L, qr), hi_ = ldsf4(L, qr + 1024);
        gst(v4u{pk2(a[0] + lo_[0], a[1] + lo_[1]), pk2(a[2] + lo_[2], a[3] + lo_[3]), pk2(a[4] + hi_[0], a[5] + hi_[1]), pk2(a[6] + hi_[2], a[7] + hi_[3])},
            rsW, l16, KVT_OFF + blk * 1024);
        publish8(ws, flag_at(FKVT + (lane & 7) * 64 + blk), epoch);
      }
      STAMP(4);
    } else {
      READ_TAG();
      unsigned warm = 0;
      {
        const GAS unsigned char* p = (const GAS unsigned char*)pc0 + (((rb & 3) * 4 + wv) * 32 + l31) * 128;
        asm volatile("global_load_dword %0, %1, off" : "+v"(warm) : "v"(p) : "memory");
      }
      __builtin_amdgcn_s_setprio(2);
      unsigned wl[4];
#pragma unroll
      for (int e = 0; e < 4; ++e) {
        const unsigned ub = min((4 * wv + e) * 56 + 32 * cb + 8 * p4, 888);
        wl[e] = RING + hh * 7168 + (ub >> 7) * 1024 + q4 * 128 + 16 * (((ub & 127) >> 4) ^ rsw) + (ub & 8);
      }
      f32x16 acc[4][2] = {};
      float sm = 0.f, cs[4] = {0.f, 0.f, 0.f, 0.f};
      v4u fx[2][2], fw[2][4], g, g2;
      const bool odd = wv & 1, sqr = wv & 2;
      unsigned ring = 0, pk;
      auto rdw = [&](int s, int e) INL { const unsigned a = wl[e] + ring; fw[s][e] = wfrag(L, a, a + 512); };
      auto rdx = [&](int s, int it) INL {
        const unsigned xp = (xa[s] ^ ((it & 1) << 6)) + (it >> 1) * SLOT;
        fx[s][0] = lds128(L, xp); fx[s][1] = lds128(L, xp + 4096);
      };
      auto step = [&](int s, int itn) INL {
        const int n = s ^ 1;
        const unsigned need = min(2 * itn + n + 1, 28);
        const v4u g0 = fx[s][0], g1 = fx[s][1];
        MM(acc[0][0], fw[s][0], g0);
        const u64 m = lcmp<0>(pk, need);
        g = v4u{odd ? g1.x : g0.x, odd ? g1.y : g0.y, odd ? g1.z : g0.z, odd ? g1.w : g0.w};
        PIN(g); SCHED();
        MM(acc[0][1], fw[s][0], g1);
        lbranch4(m, M_AL, need); rdw(n, 0); SCHED();
        MM(acc[1][0], fw[s][1], g0);
        rdw(n, 1); SCHED();
        MM(acc[1][1], fw[s][1], g1);
        rdw(n, 2); SCHED();
        MM(acc[2][0], fw[s][2], g0);
        rdw(n, 3); SCHED();
        MM(acc[2][1], fw[s][2], g1);
        rdx(n, itn); SCHED();
        MM(acc[3][0], fw[s][3], g0);
        lset(br, 2 * itn + n + 1);
        ring = ring == (NR - 1) * RSLOT ? 0 : ring + RSLOT;
        SCHED();
        MM(acc[3][1], fw[s][3], g1);
        pk = lpeek4(M_AL);
        g2 = v4u{sqr ? g.x : 0x3f803f80u, sqr ? g.y : 0x3f803f80u, sqr ? g.z : 0x3f803f80u, sqr ? g.w : 0x3f803f80u};
        PIN(g2); SCHED();
#pragma unroll
        for (int e = 0; e < 4; ++e) { cs[e] = sum8(fw[s][e], cs[e]); PIN(cs[e]); }
        { const unsigned a0 = g.x, a1 = g.y, a2 = g.z, a3 = g.w, b0 = g2.x, b1 = g2.y, b2 = g2.z, b3 = g2.w;
          sm = dot2(a3, b3, dot2(a2, b2, dot2(a1, b1, dot2(a0, b0, sm)))); PIN(sm); }
        SCHED();
      };
      lwait4f(M_AL, 1);
      STAMP(16);
      rdw(0, 0); rdw(0, 1); rdw(0, 2); rdw(0, 3); rdx(0, 0);
      lset(br, 1); ring = RSLOT;
      pk = lpeek4(M_AL);
#pragma unroll 1
      for (int it = 0; it < 14; ++it) {
        if (it == W3AT) {
          const GAS unsigned char* p = (const GAS unsigned char*)w3 + ((rb * 4 + wv) * 98 + min(lane, 48)) * 128;
          asm volatile("global_load_dword %0, %1, off\n\tglobal_load_dword %0, %2, off" : "+v"(warm) : "v"(p), "v"(p + 49 * 128) : "memory");
        }
        step(0, it);
        step(1, it + 1);
      }
      STAMP(1);
      *(LAS float*)(L + QST + wv * 128 + l31 * 4) = (sm + other_half(sm)) * (1.f / 448.f);
#pragma unroll
      for (int e = 0; e < 4; ++e) *(LAS float*)(L + QCS + (4 * wv + e) * 128 + l31 * 4) = cs[e] + other_half(cs[e]);
      waitvm<0>();
      TAKE_TAG();
      {
        const GAS unsigned char* p = (const GAS unsigned char*)w4 + ((rb * 4 + wv) * 98 + min(lane, 48)) * 128;
        const GAS unsigned char* p2 = (const GAS unsigned char*)wo + min((rb * 4 + wv) * 25 + min(lane, 24), 3135) * 128;
        asm volatile("global_load_dword %0, %1, off\n\tglobal_load_dword %0, %2, off\n\tglobal_load_dword %0, %3, off" : "+v"(warm) : "v"(p), "v"(p + 49 * 128), "v"(p2) : "memory");
      }
      bar();
      float mu[2], rr[2];
#pragma unroll
      for (int i = 0; i < 2; ++i) {
        mu[i] = *(const LAS float*)(L + QST + i * 128 + l31 * 4);
        rr[i] = __builtin_amdgcn_rsqf(fmaxf(*(const LAS float*)(L + QST + 256 + i * 128 + l31 * 4) - mu[i] * mu[i], 0.f) + LN_EPS);
      }
      v4u phi[4][2][4];
#pragma unroll
      for (int e = 0; e < 4; ++e) {
        v4u wp[2];
        load_wphi(L, (4 * wv + e) * 1568, lane, wp);
#pragma unroll
        for (int i = 0; i < 2; ++i) {
          fold_mean(L, acc[e][i], mu[i], QCS + (4 * wv + e) * 128, hh);
          feature_map(pack8(acc[e][i], 0), pack8(acc[e][i], 1), rr[i], 1.f, wp, hh, phi[e][i]);
          PIN(phi[e][i][0]); PIN(phi[e][i][1]); PIN(phi[e][i][2]); PIN(phi[e][i][3]);
        }
        SCHED();
      }
      STAMP(5);
      poll<16, 0, LGAP>(ws, flag_at(FKVT + (rb >> 2) * 64 + 16 * wv + (lane & 15)), epoch);
      asm volatile("" ::"v"(warm));
      STAMP(6);
      v4u kt[16];
#pragma unroll
      for (int i = 0; i < 16; ++i) kt[i] = gld(rsW, l16, KVT_OFF + (16 * wv + i) * 1024);
      SCHED();
      f32x16 o[2][2];
      auto omm = [&](int e, int u) INL {
        if (u == 0) { o[e & 1][0] = f32x16{}; o[e & 1][1] = f32x16{}; }
        MM(o[e & 1][0], kt[4 * e + u], phi[e][0][u]); MM(o[e & 1][1], kt[4 * e + u], phi[e][1][u]);
      };
      omm(0, 0); omm(0, 1); omm(0, 2); omm(0, 3);
#pragma unroll
      for (int e = 0; e < 4; ++e) {
#pragma unroll
        for (int i = 0; i < 2; ++i) {
          f32x16& t = o[e & 1][i];
          if (e < 3) omm(e + 1, 2 * i);
          float lo_, den;
          HALF_PAIR(t[12], t[12], lo_, den);
          const float inv = __builtin_amdgcn_rcpf(den + 1e-6f);
#pragma unroll
          for (int r = 0; r < 8; ++r) t[r] *= inv;
          const unsigned so = O_OFF + ((2 * rb + i) * 32 + 2 * (4 * wv + e)) * 1024;
          gst(pack8(t, 0), rsW, l16, so);
          SCHED();
          if (e < 3) omm(e + 1, 2 * i + 1);
#pragma unroll
          for (int r = 8; r < 16; ++r) t[r] *= inv;
          v4u u1 = pack8(t, 1);
          u1.z = hh ? 0u : u1.z; u1.w = hh ? 0u : u1.w;
          gst(u1, rsW, l16, so + 1024);
          SCHED();
        }
      }
      STAMP(7);
      publish(ws, FO + rb * 4 + wv, epoch);
      STAMP(8);
    }
    STAMP_OUT();
    return;
  }

  unsigned wn[2], wa[2];
#pragma unroll
  for (int n = 0; n < 2; ++n) {
    const unsigned sw = 16 * ((4 * n + 2 * cb + (p4 >> 1)) ^ rsw) + 8 * (p4 & 1);
    wn[n] = (8 * hh + q4) * 128 + sw;
    wa[n] = (4 * hh + q4) * 128 + sw;
  }
  auto wrap = [](int v, int n) INL { return v >= n ? v - n : v; };
  const int r3 = 2 * (7 * (rb & 1) + c), r4 = 2 * c;
  const int fj = 7 * (rb & 1) + c;
  const rsrc_t rs3 = __builtin_amdgcn_make_buffer_rsrc((void*)w3, 0, 448 * 3584, 0x00020000);

  if (wv < 4) {
    u64 epoch = 0;
    if (wv == 0) epoch = __hip_atomic_load((GAS u64*)ws, RLX_AGENT);
    __builtin_amdgcn_s_setprio(2);
    const int ti = wv >> 1, kh = wv & 1, T = 2 * rb + ti, T3 = 4 * (rb >> 1) + wv;
    const unsigned mx = M_BX + 4 * (wv ^ 1);
    const rsrc_t rs2 = __builtin_amdgcn_make_buffer_rsrc((void*)w2, 0, 448 * 896, 0x00020000);
    v4u stg[14];
    auto ld_w2 = [&](int p) INL { stg[p] = wld(rs2, vW896, (64 * (p >> 1) + 16 * wv + 8 * (p & 1)) * 896 + 128 * c); };
    float mu, rs_;
    v2u xr[4];
    {
      f32x16 acc = {};
      float sm = 0.f, cs = 0.f;
      v4u fx[2][4], fw[2][4];
      unsigned w1a[2], xg[4];
      w1a[0] = kh ? wn[1] : wn[0]; w1a[1] = w1a[0] + 65536; PIN(w1a[0]); PIN(w1a[1]);
#pragma unroll
      for (int s = 0; s < 4; ++s) { xg[s] = xa[s] + ti * 4096; PIN(xg[s]); }
      auto rdw = [&](int j, int s) INL { const unsigned o = (7 + j) * SLOT + s * 2048, a = w1a[o >> 16] + (o & 65535); fw[j & 1][s] = wfrag(L, a, a + 512); };
      auto rdx = [&](int j, int s) INL { fx[j & 1][s] = lds128(L, xg[s] + j * SLOT); };
      auto stats = [&](int j) INL {
        const v4u f0 = fx[j & 1][0], f1 = fx[j & 1][1], f2_ = fx[j & 1][2], f3 = fx[j & 1][3];
        const unsigned e0 = f0.x, e1 = f0.y, e2 = f0.z, e3 = f0.w, e4 = f1.x, e5 = f1.y, e6 = f1.z, e7 = f1.w;
        const unsigned e8 = f2_.x, e9 = f2_.y, e10 = f2_.z, e11 = f2_.w, e12 = f3.x, e13 = f3.y, e14 = f3.z, e15 = f3.w;
#define DSQ(n) "v_dot2c_f32_bf16 %0, %" #n ", %" #n "\n\t"
#define DSM(n) "v_dot2c_f32_bf16 %0, 0x3f803f80, %" #n "\n\t"
        asm volatile("s_cmp_eq_u32 %1, 0\n\ts_cbranch_scc1 1f\n\t" DSQ(2) DSQ(3) DSQ(4) DSQ(5) DSQ(6) DSQ(7) DSQ(8) DSQ(9) DSQ(10) DSQ(11) DSQ(12) DSQ(13) DSQ(14) DSQ(15) DSQ(16) DSQ(17)
                     "s_branch 2f\n1:\n\t" DSM(2) DSM(3) DSM(4) DSM(5) DSM(6) DSM(7) DSM(8) DSM(9) DSM(10) DSM(11) DSM(12) DSM(13) DSM(14) DSM(15) DSM(16) DSM(17) "\n2:"
                     : "+v"(sm) : "s"(kh), "v"(e0), "v"(e1), "v"(e2), "v"(e3), "v"(e4), "v"(e5), "v"(e6), "v"(e7), "v"(e8), "v"(e9), "v"(e10), "v"(e11), "v"(e12), "v"(e13), "v"(e14), "v"(e15) : "scc");
        SCHED();
      };
      auto csum = [&](int j) INL {
#pragma unroll
        for (int s = 0; s < 4; ++s) cs = sum8(fw[j & 1][s], cs);
        PIN(cs);
        SCHED();
      };
      waitvm<0>();
      if (wv == 0) TAKE_TAG();
      lset(M_BS + 4 * wv, 1);
      lwait4f(M_BS, 1);
      STAMP(16);
#pragma unroll
      for (int s = 0; s < 4; ++s) { rdw(0, s); rdx(0, s); }
      lset(br, 1);
      unsigned pk = lpeek4(M_AL);
      u64 m = lcmp<0>(pk, 5);
      SCHED();
#pragma unroll
      for (int j = 0; j < 5; ++j) {
        MM(acc, fw[j & 1][0], fx[j & 1][0]);
        lbranch4(m, M_AL, 2 * j + 5);
        rdw(j + 1, 0); SCHED();
        MM(acc, fw[j & 1][1], fx[j & 1][1]);
        rdw(j + 1, 1); rdw(j + 1, 2);
        SCHED();
        MM(acc, fw[j & 1][2], fx[j & 1][2]);
        rdw(j + 1, 3); lset(br, j + 2);
        SCHED();
        MM(acc, fw[j & 1][3], fx[j & 1][3]);
        rdx(j + 1, 0); rdx(j + 1, 1); rdx(j + 1, 2); rdx(j + 1, 3);
        if (j < 4) pk = lpeek4(M_AL);
        SCHED();
        stats(j);
        csum(j);
        if (j < 4) m = lcmp<0>(pk, 2 * j + 7);
        SCHED();
      }
      MM(acc, fw[1][0], fx[1][0]);
      rdx(6, 0); rdx(6, 1); SCHED();
      MM(acc, fw[1][1], fx[1][1]);
      rdx(6, 2); rdx(6, 3); SCHED();
      MM(acc, fw[1][2], fx[1][2]);
#pragma unroll
      for (int g = 0; g < 4; ++g) xr[g] = *(const LAS v2u*)(L + c * SLOT + (32 * ti + l31) * 128 + 16 * ((4 * kh + g) ^ ((l31 >> 1) & 7)) + 8 * hh);
      SCHED();
      MM(acc, fw[1][3], fx[1][3]);
      stats(5); stats(6);
      pk = lpeek4(M_AL);
      csum(5);
      *(LAS float*)(L + M_ST + wv * 128 + l31 * 4) = (sm + other_half(sm)) * (1.f / 448.f);
      lset(M_BL + 4 * wv, 1);
      m = lcmp<2>(pk, 14);
      pk = lpeek4(M_BL);
      lbranch4(m, M_AL, 14);
      rdw(6, 0); rdw(6, 1); rdw(6, 2); rdw(6, 3);
      lset(br, 7);
      m = lcmp<9>(pk, 1);
      lbranch4(m, M_BL, 1);
      epoch = *(const LAS u64*)(L + M_TAG);
      mu = *(const LAS float*)(L + M_ST + (wv & 2) * 128 + l31 * 4);
      const float ex2 = *(const LAS float*)(L + M_ST + (wv | 1) * 128 + l31 * 4);
      SCHED();
      csum(6);
      MM(acc, fw[0][0], fx[0][0]);
      *(LAS float*)(L + M_CS1 + wv * 128 + l31 * 4) = cs + other_half(cs);
      SCHED();
      MM(acc, fw[0][1], fx[0][1]);
      v4f c4[4];
#pragma unroll
      for (int g = 0; g < 4; ++g) c4[g] = ldsf4(L, M_CS1 + wv * 128 + g * 32 + hh * 16);
      const float var = fmaxf(ex2 - mu * mu, 0.f);
      rs_ = __builtin_amdgcn_rsqf(var + LN_EPS);
      PIN(rs_);
      SCHED();
      MM(acc, fw[0][2], fx[0][2]);
      const float v2 = var * rs_ * rs_ + LN_EPS, r2 = __builtin_amdgcn_rsqf(v2);
      GeluK gk = gelu_k(rs_ * r2, (var + LN_EPS) * rs_ * v2 * r2);
      PIN(gk.k1); PIN(gk.k2); PIN(gk.ir);
      SCHED();
      MM(acc, fw[0][3], fx[0][3]);
      {
        const unsigned o = (32 * ti + l31) * 4;
        *(LAS float*)(L + M_MU + o) = mu; *(LAS float*)(L + M_R + o) = rs_; *(LAS float*)(L + M_IR + o) = (var + LN_EPS) * rs_;
        lset(M_BS + 4 * wv, 2);
      }
      SCHED();
      STAMP(2);
#pragma unroll
      for (int r = 0; r < 16; ++r) acc[r] = gelu(__builtin_fmaf(-mu, c4[r >> 2][r & 3], acc[r]), gk);
      const unsigned so = H_OFF + (T * 28 + 4 * c + 2 * kh) * 1024;
      gst(pack8(acc, 0), rsW, l16, so);
      gst(pack8(acc, 1), rsW, l16, so + 1024);
      STAMP(17);
      publish(ws, FH + (rb * 7 + c) * 4 + wv, epoch);
    }
    STAMP(3);
    v2u x1p[4];
#pragma unroll
    for (int p = 0; p < 14; ++p) ld_w2(p);
    SCHED();
    unsigned wk[2][3];
#pragma unroll
    for (int i = 0; i < 3; ++i) { wk[0][i] = (kh ? wa[1] : wa[0]) + 65536 * i; wk[1][i] = (kh ? wa[0] : wa[1]) + 65536 * i; PIN(wk[0][i]); PIN(wk[1][i]); }
    auto wacc = [&](int n, unsigned off) INL { const unsigned a = wk[n][off >> 16] + (off & 65535); return wfrag(L, a, a + 1024); };
    unsigned f_h, f_x, f_h2, v0, v1;
    {
      const int l15 = min(lane, 14);
      const int tl = l15 < 7 ? T : l15 < 11 ? max(T - 1, 0) : min(T + 1, 63);
      const int nt = 7 * kh + (l15 < 7 ? l15 : l15 < 11 ? l15 - 7 : l15 - 8);
      f_h = FH + ((tl >> 1) * 7 + (nt >> 1)) * 4 + 2 * (tl & 1) + (nt & 1);
      f_x = FX + ((T3 >> 1) * 7 + min(lane >> 1, 6)) * 4 + 2 * (T3 & 1) + (lane & 1);
      f_h2 = FH2 + ((2 * (rb >> 1) + kh) * 7 + min(lane, 6)) * 4 + (T & 3);
      const int tok = 32 * T + l31, m0_ = tok - 1 - kh, m1_ = tok + 1 + kh;
      v0 = (m0_ >> 5) * 28672 + ((m0_ & 31) + 32 * hh) * 16; v0 = m0_ >= 0 ? v0 : 0x80000000u;
      v1 = (m1_ >> 5) * 28672 + ((m1_ & 31) + 32 * hh) * 16; v1 = m1_ < 2048 ? v1 : 0x80000000u;
      f_h = WS_FLAG + f_h * FLAG_STRIDE; f_x = WS_FLAG + f_x * FLAG_STRIDE; f_h2 = WS_FLAG + f_h2 * FLAG_STRIDE;
      PIN(f_h); PIN(f_x); PIN(f_h2); PIN(v0); PIN(v1);
    }
    {
      f32x16 y, snd = {};
      const float nm = -mu * rs_;
#pragma unroll
      for (int g = 0; g < 4; ++g) {
        const unsigned u0 = xr[g].x, u1 = xr[g].y;
        y[4 * g] = __builtin_fmaf(bflo(u0), rs_, nm); y[4 * g + 1] = __builtin_fmaf(bfhi(u0), rs_, nm);
        y[4 * g + 2] = __builtin_fmaf(bflo(u1), rs_, nm); y[4 * g + 3] = __builtin_fmaf(bfhi(u1), rs_, nm);
      }
      unsigned w2a[2][2];
#pragma unroll
      for (int n = 0; n < 2; ++n) {
        w2a[n][0] = wk[n][0] + (kh ? 7 * SLOT + 4096 : 16 * SLOT); w2a[n][1] = wk[n][0] + (kh ? 7 * SLOT + 4096 + 12 * 2048 : 7 * SLOT);
        PIN(w2a[n][0]); PIN(w2a[n][1]);
      }
      PIN(y);
      poll<15, PRE1, 0>(ws, f_h, epoch);
      STAMP(4);
      const unsigned sb = H_OFF + 14 * kh * 1024;
      v4u hf[14];
#pragma unroll
      for (int s = 0; s < 14; ++s) hf[s] = gld(rsW, s < 7 ? v0 : v1, sb + s * 1024);
      SCHED();
      lwait4f(M_AR, 5);
      STAMP(1);
#pragma unroll
      for (int p = 0; p < 14; ++p) *(LAS v4u*)(L + opq(l16 + wv * 2048 + BSLOT[p >> 1] * SLOT) + (p & 1) * 1024) = stg[p];
      lset(M_BL + 4 * wv, 2);
      lwait4f(M_BL, 2);
      STAMP(18);
      auto w2f = [&](int n, int s) INL { const unsigned a = s < 12 ? w2a[n][0] + s * 2048 : w2a[n][1] + (s - 12) * 2048; return wfrag(L, a, a + 1024); };
      v4u f0[3], f1[3];
      f0[0] = w2f(0, 0); f1[0] = w2f(1, 0); f0[1] = w2f(0, 1); f1[1] = w2f(1, 1);
      unsigned pk = 0;
#pragma unroll
      for (int s = 0; s < 14; ++s) {
        MM(snd, f1[s % 3], hf[s]);
        if (s < 12) f1[(s + 2) % 3] = w2f(1, s + 2);
        if (s < 7) stg[2 * s] = wld(rs3, vW3584, 16 * wrap(4 * s + wv + r3, 28) * 3584 + 256 * fj);
        SCHED();
        MM(y, f0[s % 3], hf[s]);
        if (s < 12) f0[(s + 2) % 3] = w2f(0, s + 2);
        if (s < 7) stg[2 * s + 1] = wld(rs3, vW3584, (16 * wrap(4 * s + wv + r3, 28) + 8) * 3584 + 256 * fj);
        if (s == 11) lset(br, 8);
        if (s == 12) pk = lpeek4(M_BR);
        SCHED();
      }
      STAMP(5);
      lbranch4(lcmp<0>(pk, 8), M_BR, 8);
#pragma unroll
      for (int g = 0; g < 4; ++g) *(LAS v4f*)(L + XCH + wv * 4096 + g * 1024 + l16) = v4f{snd[4 * g], snd[4 * g + 1], snd[4 * g + 2], snd[4 * g + 3]};
      lset(M_BX + 4 * wv, 1);
      lwait<0, 0>(mx, 1);
#pragma unroll
      for (int g = 0; g < 4; ++g) {
        const v4f o = ldsf4(L, XCH + (wv ^ 1) * 4096 + g * 1024 + l16);
        x1p[g] = v2u{pk2(y[4 * g] + o[0], y[4 * g + 1] + o[1]), pk2(y[4 * g + 2] + o[2], y[4 * g + 3] + o[3])};
      }
      lset(br, 9);
      const unsigned so = X1_OFF + (T * 28 + 4 * c + 2 * kh) * 1024;
      const v4u p0 = v4u{x1p[0].x, x1p[0].y, x1p[1].x, x1p[1].y}, p1 = v4u{x1p[2].x, x1p[2].y, x1p[3].x, x1p[3].y};
      gst(p0, rsW, l16, so);
      gst(p1, rsW, l16, so + 1024);
      const float sm = sum8(p1, sum8(p0, 0.f)), sq = sq8(p1, sq8(p0, 0.f));
      float s1, s2;
      HALF_PAIR(sm, sq, s1, s2);
      __builtin_amdgcn_raw_buffer_store_b32(f2u(s1 + s2), rsW, XS_OFF + (T * 14 + 2 * c + kh) * 256 + lane * 4, 0, 16);
      STAMP(19);
      publish(ws, FX + (rb * 7 + c) * 4 + wv, epoch);
    }
    STAMP(6);
    {
      auto put3 = [&](int p) INL { *(LAS v4u*)(L + opq(l16 + wv * 2048 + CSLOT[p >> 1] * SLOT) + (p & 1) * 1024) = stg[p]; };
      lwait4f(M_BR, 9);
      put3(0); put3(1); put3(2); put3(3);
      lset(M_BL + 4 * wv, 3);
      poll<14, PRE2, 0>(ws, f_x, epoch);
      STAMP(7);
      constexpr int XW = 12;
      v4u xf[28];
      float ps[14];
#pragma unroll
      for (int s = 0; s < XW; ++s) xf[s] = gld(rsW, l16, X1_OFF + (T3 * 28 + wrap(s + r3, 28)) * 1024);
      unsigned pk = lpeek4(M_BL);
      SCHED();
      lwait4f(M_AR, 12);
#pragma unroll
      for (int p = 4; p < 14; ++p) put3(p);
      lset(M_BL + 4 * wv, 4);
      lbranch4(lcmp<11>(pk, 3), M_BL, 3);
      STAMP(21);
      f32x16 t[4] = {};
      auto w3f = [&](int n, int s) INL { return wacc(n & 1, CSLOT[7 * (n >> 1) + (s >> 2)] * SLOT + (s & 3) * 2048); };
      float c0s = 0.f, c1s = 0.f;
      {
        v4u f0[3], f1[3];
        f0[0] = w3f(0, 0); f1[0] = w3f(1, 0); f0[1] = w3f(0, 1); f1[1] = w3f(1, 1);
        sfor<0, 28>([&](auto sc) INL {
          constexpr int s = decltype(sc)::value;
          sum8x2_if(wv, (s >> 2) & 3, f0[s % 3], f1[s % 3], c0s, c1s);
          MM(t[0], f0[s % 3], xf[s]);
          if (s == 6) lbranch4(lcmp<8>(pk, 4), M_BL, 4);
          if (s < 26) f0[(s + 2) % 3] = w3f(0, s + 2);
          SCHED();
          MM(t[1], f1[s % 3], xf[s]);
          if (s == 3) pk = lpeek4(M_BL);
          if (s < 26) f1[(s + 2) % 3] = w3f(1, s + 2);
          if constexpr (s + XW < 28) xf[s + XW] = gld(rsW, l16, X1_OFF + (T3 * 28 + wrap(s + XW + r3, 28)) * 1024);
          else if constexpr (s < 23) {
#pragma unroll
            for (int i = 2 * (s - 16); i < 2 * (s - 16) + 2; ++i) ps[i] = u2f(__builtin_amdgcn_raw_buffer_load_b32(rsW, lane * 4, XS_OFF + (T3 * 14 + i) * 256, 16));
          }
          SCHED();
        });
      }
      lset(br, 10);
      STAMP(8);
      pk = lpeek4(M_AL);
      {
        float a_, b_;
        HALF_PAIR(c0s, c1s, a_, b_);
        *(LAS float*)(L + (wv < 2 ? M_CSP0 : M_CSP2 - 512) + wv * 256 + (lane ^ (32 * kh)) * 4) = a_ + b_;
        lset(M_BC + 4 * wv, 1);
      }
      lbranch4(lcmp<2>(pk, 25), M_AL, 25);
      STAMP(20);
      const unsigned so = l16 + H2_OFF + (T3 * 112 + 8 * fj) * 1024 + kh * 2048, csb = M_CS3 + hh * 16 + kh * 128;
      const unsigned so1 = so - kh * 4096 + 2048, csb1 = csb - kh * 256 + 128;
      float mu1;
      GeluK gk;
      v4f cs[2];
      float ga[3], ge[2], gr[2];
      auto ldcs = [&](int g) INL { cs[g & 1] = ldsf4(L, ((g >> 2) & 1 ? csb1 : csb) + (g >> 3) * 256 + (g & 3) * 32); };
      auto epi = [&](int e) INL {
        if (e < 64) {
          if ((e & 3) == 0 && e + 4 < 64 && e + 4 != 32) ldcs((e >> 2) + 1);
          const float a = __builtin_fmaf(-mu1, cs[(e >> 2) & 1][e & 3], t[e >> 4][e & 15]);
          ga[e % 3] = a;
          ge[e & 1] = __builtin_amdgcn_exp2f(a * __builtin_fmaf(a * a, gk.k2, gk.k1));
          PIN(ga[e % 3]); PIN(ge[e & 1]);
        }
        if (e >= 1 && e < 65) { gr[(e - 1) & 1] = __builtin_amdgcn_rcpf(__builtin_fmaf(ge[(e - 1) & 1], gk.ir, gk.ir)); PIN(gr[(e - 1) & 1]); }
        if (e >= 2) {
          const int d = e - 2, n = d >> 4, r = d & 15;
          t[n][r] = ga[d % 3] * gr[d & 1];
          if ((r & 7) == 7) __builtin_amdgcn_raw_buffer_store_b128(pack8(t[n], r >> 3), rsW, ((n & 1) ? so1 : so) + ((n >> 1) * 4 + (r >> 3)) * 1024, 0, 16);
          else { float tmp = t[n][r]; PIN(tmp); t[n][r] = tmp; }
        }
      };
      {
        v4u f[4];
        f[0] = w3f(2, 0); f[1] = w3f(2, 1); f[2] = w3f(2, 2);
        float p_[4], tot;
        sfor<0, 56>([&](auto kc) INL {
          constexpr int k = decltype(kc)::value;
          MM(t[2 + k / 28], f[k % 4], xf[k % 28]);
          if constexpr (k + 3 < 56) f[(k + 3) % 4] = w3f(2 + (k + 3) / 28, (k + 3) % 28);
          if (k == 0) { tot = (ps[0] + ps[1]) + (ps[2] + ps[3]) + (ps[4] + ps[5]); PIN(tot); pk = lpeek4(M_BC); }
          if (k == 1) { tot += (ps[6] + ps[7]) + (ps[8] + ps[9]) + (ps[10] + ps[11]); PIN(tot); }
          if (k == 2) { tot = (tot + (ps[12] + ps[13])) * (1.f / 448.f); PIN(tot); }
          if (k == 3) {
            lbranch4(lcmp<6>(pk, 1), M_BC, 1);
            p_[0] = *(const LAS float*)(L + M_CSP0 + lane * 4); p_[1] = *(const LAS float*)(L + M_CSP0 + 256 + lane * 4);
            p_[2] = *(const LAS float*)(L + M_CSP2 + lane * 4); p_[3] = *(const LAS float*)(L + M_CSP2 + 256 + lane * 4);
          }
          if (k == 4) { float m2; HALF_PAIR(tot, tot, mu1, m2); tot = fmaxf(m2 - mu1 * mu1, 0.f) + LN_EPS; PIN(tot); PIN(mu1); }
          if (k == 5) { const float r1 = __builtin_amdgcn_rsqf(tot); gk = gelu_k(r1, tot * r1); PIN(gk.k1); PIN(gk.k2); PIN(gk.ir); }
          if (k == 6) { *(LAS float*)(L + M_CS3 + lane * 4) = (p_[0] + p_[1]) + (p_[2] + p_[3]); ldcs(0); }
          if (k == 34) pk = lpeek<M_ACS - MISC>(m4 & ~8u);
          if (k == 38) { lbranch<M_ACS - MISC>(lcmp<8>(pk, 1), m4 & ~8u, 1); ldcs(8); }
          if constexpr (k >= 9) epi(k - 9);
          if (k == 52) lset(br, 11);
          SCHED();
        });
      }
#pragma unroll
      for (int e = 47; e < 66; ++e) { epi(e); SCHED(); }
      STAMP(9);
      publish(ws, FH2 + (rb * 7 + c) * 4 + wv, epoch);
    }
    STAMP(10);
    {
      f32x16 gt, snd = {};
#pragma unroll
      for (int g = 0; g < 4; ++g) {
        const unsigned a0 = x1p[g].x, a1 = x1p[g].y;
        gt[4 * g] = bflo(a0); gt[4 * g + 1] = bfhi(a0); gt[4 * g + 2] = bflo(a1); gt[4 * g + 3] = bfhi(a1);
      }
      PIN(gt);
      constexpr int PRE = 12;
      v4u p0[PRE], p1[PRE];
      lwait4f(M_AL, d_al(2));
#pragma unroll
      for (int u = 0; u < PRE / 4; ++u) {
        const unsigned o = DRING[2 * u] * SLOT + kh * ((DRING[2 * u + 1] - DRING[2 * u]) * SLOT);
        const unsigned a0 = opq(wk[0][0] + o), a1 = opq(wk[1][0] + o);
#pragma unroll
        for (int s = 0; s < 4; ++s) { p0[4 * u + s] = wfrag(L, a0 + s * 2048, a0 + s * 2048 + 1024); p1[4 * u + s] = wfrag(L, a1 + s * 2048, a1 + s * 2048 + 1024); }
        lset(br, 12 + u);
        SCHED();
      }
      poll<7, PRE3, 0>(ws, f_h2, epoch);
      STAMP(11);
      constexpr int WIN = 16;
      const unsigned sb = H2_OFF + (T * 112 + 56 * kh) * 1024, ob = O_OFF + (T * 32 + 16 * kh) * 1024;
      v4u af[WIN];
      auto act = [&](int j) INL { af[j % WIN] = gld(rsW, l16, j < 56 ? sb + wrap(j + 4 * r4, 56) * 1024 : ob + (j - 56) * 1024); };
#pragma unroll
      for (int j = 0; j < WIN; ++j) act(j);
      SCHED();
      const unsigned xo = XCH4 + wv * SLOT + l16, xi = XCH4 + (wv ^ 1) * SLOT + l16, mor = M_OR;
      unsigned db[2][2], e0[2], e1[2];
      auto dbase = [&](int u) INL {
        const unsigned o = DRING[(2 * u) % 12] * SLOT + kh * ((DRING[(2 * u + 1) % 12] - DRING[(2 * u) % 12]) * SLOT);
        db[u & 1][0] = wk[0][0] + o; db[u & 1][1] = wk[1][0] + o; PIN(db[u & 1][0]); PIN(db[u & 1][1]);
      };
#pragma unroll
      for (int n = 0; n < 2; ++n) { e0[n] = wk[n][0] + kh * 224 * 128; e1[n] = e0[n] + (hh ? 20 * 128 : 24 * 128); PIN(e0[n]); PIN(e1[n]); }
      auto wf = [&](int n, int k) INL {
        if (k < 56) { const unsigned a = db[(k >> 2) & 1][n] + (k & 3) * 2048; return wfrag(L, a, a + 1024); }
        const int s = k - 56;
        const unsigned ro = (28 * (s >> 1) + 16 * (s & 1)) * 128;
        return wfrag(L, e0[n] + ro, (s & 1) ? e1[n] + 28 * (s >> 1) * 128 : e0[n] + ro + 1024);
      };
      v4u f0[3], f1[3];
      f32x16 s0 = {}, s1 = {};
      v4f oy[4];
      float ge[2];
      auto gate = [&](int e) INL {
        if (e < 16) { gt[e] += oy[e >> 2][e & 3]; ge[e & 1] = __builtin_amdgcn_exp2f(-LOG2E * gt[e]); PIN(ge[e & 1]); }
        if (e >= 1) { float v = gt[e - 1] * __builtin_amdgcn_rcpf(1.0f + ge[(e - 1) & 1]); PIN(v); gt[e - 1] = v; }
      };
      unsigned pk = 0, pk2_ = 0;
      u64 m = 0, m2 = 0;
      sfor<0, 72>([&](auto kc) INL {
        constexpr int k = decltype(kc)::value, un = (k + 2) >> 2;
        constexpr bool first = ((k + 2) & 3) == 0 && un >= 6 && un < 14;
        if constexpr (k < PRE) MM(snd, p1[k], af[k % WIN]); else if constexpr (k < 56) MM(snd, f1[k % 3], af[k % WIN]); else MM(s1, f1[k % 3], af[k % WIN]);
        if constexpr (first) lbranch4(m, M_AL, d_al(un));
        if constexpr (k + 2 >= PRE && k + 2 < 72) f1[(k + 2) % 3] = wf(1, k + 2);
        if constexpr ((k & 3) == 3 && (k + 5) / 4 >= 6 && (k + 5) / 4 < 14) pk = lpeek4(M_AL);
        if constexpr ((k & 3) == 0 && k + 4 >= PRE && k / 4 + 1 < 14) dbase(k / 4 + 1);
        if constexpr (k == 37) pk2_ = lpeek<0>(mor);
        if constexpr (k == 52) pk2_ = lpeek4(M_BR);
        if constexpr (k == 57) {
          lbranch4(m2, M_BR, 22);
          *(LAS v4f*)(L + xo) = v4f{snd[0], snd[1], snd[2], snd[3]}; *(LAS v4f*)(L + xo + 1024) = v4f{snd[4], snd[5], snd[6], snd[7]};
        }
        if constexpr (k == 59) pk2_ = lpeek<0>(mx);
        if constexpr (k == 61) {
          lbranch<0>(lcmp<8>(pk2_, 2), mx, 2);
          oy[0] = ldsf4(L, xi); oy[1] = ldsf4(L, xi + 1024);
        }
        if constexpr (k >= 63) gate(2 * (k - 63));
        SCHED();
        if constexpr (k < PRE) MM(gt, p0[k], af[k % WIN]); else if constexpr (k < 56) MM(gt, f0[k % 3], af[k % WIN]); else MM(s0, f0[k % 3], af[k % WIN]);
        if constexpr (k + 2 >= PRE && k + 2 < 72) f0[(k + 2) % 3] = wf(0, k + 2);
        if constexpr ((k & 3) == 1 && k >= PRE && k < 56) lset(br, 12 + k / 4);
        if constexpr ((k & 3) == 1 && (k + 3) / 4 >= 6 && (k + 3) / 4 < 14) m = lcmp<11>(pk, d_al((k + 3) / 4));
        if constexpr (k == 39) m2 = lcmp<10>(pk2_, 1);
        if constexpr (k == 40) { STAMP(12); lbranch<0>(m2, mor, 1); STAMP(13); }
        if constexpr (k == 54) m2 = lcmp<8>(pk2_, 22);
        if constexpr (k == 57) {
          *(LAS v4f*)(L + xo + 2048) = v4f{snd[8], snd[9], snd[10], snd[11]}; *(LAS v4f*)(L + xo + 3072) = v4f{snd[12], snd[13], snd[14], snd[15]};
          lset(M_BX + 4 * wv, 2);
        }
        if constexpr (k == 61) { oy[2] = ldsf4(L, xi + 2048); oy[3] = ldsf4(L, xi + 3072); }
        if constexpr (k >= 63 && k < 71) gate(2 * (k - 63) + 1);
        if constexpr (k + WIN < 72) act(k + WIN);
        if constexpr (k == 55) STAMP(22);
        SCHED();
      });
      STAMP(23);
#pragma unroll
      for (int g = 0; g < 4; ++g) *(LAS v4f*)(L + xo + 4096 + g * 1024) = v4f{s1[4 * g], s1[4 * g + 1], s1[4 * g + 2], s1[4 * g + 3]};
      lset(M_BX + 4 * wv, 3);
      pk = lpeek<0>(mx);
      f32x16 r;
#pragma unroll
      for (int g = 0; g < 4; ++g) {
        const unsigned u0 = xr[g].x, u1 = xr[g].y;
        r[4 * g] = __builtin_fmaf(s0[4 * g], gt[4 * g], bflo(u0)); r[4 * g + 1] = __builtin_fmaf(s0[4 * g + 1], gt[4 * g + 1], bfhi(u0));
        r[4 * g + 2] = __builtin_fmaf(s0[4 * g + 2], gt[4 * g + 2], bflo(u1)); r[4 * g + 3] = __builtin_fmaf(s0[4 * g + 3], gt[4 * g + 3], bfhi(u1));
      }
      PIN(r);
      lbranch<0>(lcmp<0>(pk, 3), mx, 3);
      v2u op[4];
#pragma unroll
      for (int g = 0; g < 4; ++g) {
        const v4f os = ldsf4(L, xi + 4096 + g * 1024);
        op[g] = v2u{pk2(__builtin_fmaf(os[0], gt[4 * g], r[4 * g]), __builtin_fmaf(os[1], gt[4 * g + 1], r[4 * g + 1])),
                    pk2(__builtin_fmaf(os[2], gt[4 * g + 2], r[4 * g + 2]), __builtin_fmaf(os[3], gt[4 * g + 3], r[4 * g + 3]))};
      }
      unsigned char* orow = (unsigned char*)out + (size_t)(32 * T + l31) * 896 + 128 * c + 64 * kh + 16 * hh;
#pragma unroll
      for (int g = 0; g < 4; g += 2) {
        const auto e0_ = __builtin_amdgcn_permlane32_swap(op[g].x, op[g + 1].x, false, false);
        const auto e1_ = __builtin_amdgcn_permlane32_swap(op[g].y, op[g + 1].y, false, false);
        const unsigned p0 = e0_[0], p1 = e1_[0], p2 = e0_[1], p3 = e1_[1];
        *(GAS v4u*)(orow + 16 * g) = v4u{p0, p1, p2, p3};
      }
    }
    STAMP(14);
    STAMP_OUT();
    return;
  }

  {
    const int h0 = c < 4 ? 2 * c : 2 * c + 1, nh = (c == 3 || c == 6) ? 3 : 2, line0 = c ? c - 1 : 0;
    const unsigned kcol = 896 + 128 * line0;
    auto dma_k2 = [&](int it) INL {
      dman<2, 8 * 2688>(KSLOT[it] * SLOT + aw * 2048, vW2688, rsQ, (64 * (it >> 1) + 16 * aw) * 2688 + kcol + 128 * (it & 1));
    };
    constexpr int LAG = XLAG;
#pragma unroll
    for (int m = 2; m < 17; ++m) {
      if (m < 9) { if (m & 1) dma_x((m + 3) >> 1); else dma_w1((m + 2) >> 1); }
      else if (m == 9) dma_x(6);
      else if (m == 10) dma_w1(6);
      else if (m < 16) dma_k2(m - 11);
      else {
        dma(M_PHI + min(2 * aw, 4) * 1024, l16 + h0 * 1568 + min(2 * aw, 4) * 1024, rsP, 0);
        dma(M_PHI + min(2 * aw + 1, 4) * 1024, l16 + h0 * 1568 + min(2 * aw + 1, 4) * 1024, rsP, 0);
      }
      const int d = m - LAG;
      if (d == 0 || d == 2 || d == 4 || d == 6 || d == 9 || d == 10) { waitvm<2 * LAG>(); lset(al, d < 9 ? d + 5 : d + 4); }
    }
    STAMP(16);
    static_assert(LAG >= 2 && LAG <= 10, "lag");
    if (LAG > 7) { waitvm<14>(); lset(al, 13); }
    if (LAG > 6) { waitvm<12>(); lset(al, 14); }
    STAMP(17);
    typedef const GAS unsigned char* gp;
    const int cu = rb * 7 + c, i14 = cu * 14 + lane % 14;
    unsigned warm = 0;
    {
      gp p1 = lane >= 52 ? (gp)ws + WS_FLAG + ((lane - 52) / 4 * 896 + cu * 4 + (lane & 3)) * FLAG_STRIDE : (gp)ws + min(lane, 35) * 491520;
      p1 = lane == 51 ? (gp)out + rb * 57344 : p1;
      const gp p2 = lane >= 14 ? (gp)wqkv + (i14 / 7) * 2688 + 1792 + (i14 % 7) * 128 : (gp)w2 + i14 * 128;
      p1 = aw == 1 ? p1 : aw == 2 ? p2 : (gp)ws;
      asm volatile("global_load_dword %0, %1, off sc1" : "+v"(warm) : "v"(p1) : "memory");
    }
    lwait4f(M_BR, 4);
#pragma unroll
    for (int it = 5; it < 9; ++it) dma_k2(it);
    waitvm<8>(); lset(al, 15);
    const int head = h0 + aw;
    const unsigned ub = min(head * 56 - line0 * 128 + 32 * cb + 8 * p4, 248);
    unsigned kl = (8 * hh + q4) * 128 + 16 * (((ub & 127) >> 4) ^ rsw) + (ub & 8), ln = ub >> 7;
    unsigned klv = (cb && p4 == 3) ? 0x30000u : kl;
    v4u id[2] = {ident(0, lane), ident(1, lane)};
    PIN(id[0]); PIN(id[1]); PIN(kl); PIN(klv); PIN(ln);

    asm volatile("" ::"v"(warm));
    lwait4f(M_BR, 7);
#pragma unroll
    for (int it = 9; it < 12; ++it) dma_k2(it);
    STAMP(2);
    auto w3b_issue = [&]() INL {
      lwait4f(M_AR, 14);
#pragma unroll
      for (int j = 0; j < 7; ++j) dman<2, 8 * 3584>(j * SLOT + aw * 2048, vW3584, rs3, 16 * wrap(4 * j + aw + r3, 28) * 3584 + 256 * fj + 128);
    };
    auto v_last = [&]() INL {
#pragma unroll
      for (int it = 10; it < 14; ++it) dman<2, 8 * 2688>(VSLOT[it] * SLOT + aw * 2048, vW2688, rsQ, (64 * (it >> 1) + 16 * aw) * 2688 + kcol + 896 + 128 * (it & 1));
    };
    auto w3b = [&]() INL { w3b_issue(); waitvm<0>(); lset(al, 25); };
    if (aw == 3) {
      auto kv8 = [&](int slot, int it, unsigned col) INL { dman<8, 8 * 2688>(slot * SLOT, vW2688, rsQ, 64 * (it >> 1) * 2688 + col + 128 * (it & 1)); };
      auto v8 = [&](int it) INL { kv8(VSLOT[it], it, kcol + 896); };
      lset(ar, 14);
      waitvm<6>(); lset(al, 16);
      lwait4(M_AR, 1); kv8(KSLOT[12], 12, kcol); kv8(KSLOT[13], 13, kcol);
      waitvm<16>(); lset(al, 17);
      waitvm<0>(); lset(al, 18);
      lwait4(M_AR, 6); v8(0); v8(1);
      v8(2);
      lwait4(M_AR, 7); v8(3); v8(4);
      waitvm<0>(); lset(al, 19);
      lwait4(M_BR, 8); v8(5); v8(6); v8(7);
      waitvm<16>(); lset(al, 20);
      v8(8); v8(9);
      waitvm<16>(); lset(al, 21);
      lwait4(M_AR, 9); v_last();
      waitvm<8>(); lset(al, 22);
      waitvm<0>(); lset(al, 24);
      STAMP(3);
      w3b();
      STAMP(4);
    } else if (aw >= nh) {
      lset(ar, 14);
      waitvm<6>(); lset(al, 16);
      waitvm<0>(); lset(al, 22);
      lwait4(M_AR, 9); v_last();
      waitvm<0>(); lset(al, 24);
      w3b();
    } else {
      v4u f[2][4], x0[2][4], x1[2][4];
      auto rd = [&](int j, bool isv, int i) INL {
        const int s0 = isv ? VSLOT[2 * j] : KSLOT[2 * j], s1 = isv ? VSLOT[2 * j + 1] : KSLOT[2 * j + 1], s = i >> 1;
        if (i & 1) { x0[j & 1][s] = lds128(L, xa[s] + j * SLOT); x1[j & 1][s] = lds128(L, xa[s] + j * SLOT + 4096); }
        else { const unsigned base = opq((isv ? klv : kl) + s0 * SLOT + ln * ((s1 - s0) * SLOT)); f[j & 1][s] = wfrag(L, base + s * 2048, base + s * 2048 + 512); }
        if (i == 7) lset(ar, (isv ? 8 : 1) + j);
      };
      float cs = 0.f;
      u64 am = ~0ull;
      auto chunk = [&](int j, bool isv, f32x16& u0, f32x16& u1, int need_al, int next_al) INL {
        const int p = j & 1;
#pragma unroll
        for (int g = 0; g < 8; ++g) {
          const int s = g >> 1;
          if (g & 1) { if (isv) MM(u1, x1[p][s], f[p][s]); else MM(u1, f[p][s], x1[p][s]); }
          else { if (isv) MM(u0, x0[p][s], f[p][s]); else MM(u0, f[p][s], x0[p][s]); }
          if (g == 0 && need_al) lbranch4(am, M_AL, need_al);
          if (j < 6) rd(j + 1, isv, g);
          SCHED();
        }
        unsigned apk = 0;
        if (next_al) apk = lpeek4(M_AL);
#pragma unroll
        for (int s = 0; s < 4; ++s) cs = sum8(f[p][s], cs);
        PIN(cs);
        if (next_al) am = lcmp<0>(apk, next_al);
        SCHED();
      };
      f32x16 a0 = {}, a1 = {};
      v4u wp[2];
      __builtin_amdgcn_s_setprio(KPRIO);
      lwait4f(M_AL, 15);
#pragma unroll
      for (int i = 0; i < 8; ++i) rd(0, false, i);
      chunk(0, false, a0, a1, 0, 0); waitvm<6>(); lset(al, 16);
      chunk(1, false, a0, a1, 16, 0);
      waitvm<0>(); lset(al, 22);
      chunk(2, false, a0, a1, 0, 17);
      chunk(3, false, a0, a1, 17, 0);
      chunk(4, false, a0, a1, 0, 18);
      chunk(5, false, a0, a1, 18, 0);
      load_wphi(L, M_PHI + aw * 1568, lane, wp);
      chunk(6, false, a0, a1, 0, 0);
      STAMP(3);
      *(LAS float*)(L + M_CSK + aw * 128 + l31 * 4) = cs + other_half(cs);
      lwait4f(M_BS, 2);
      const u64 epoch = *(const LAS u64*)(L + M_TAG);
      const float r0 = *(const LAS float*)(L + M_R + l31 * 4), r1 = *(const LAS float*)(L + M_R + 128 + l31 * 4);
      v4u kf[2][2];
      fold_mean(L, a0, *(const LAS float*)(L + M_MU + l31 * 4), M_CSK + aw * 128, hh);
      fold_mean(L, a1, *(const LAS float*)(L + M_MU + 128 + l31 * 4), M_CSK + aw * 128, hh);
      kf[0][0] = pack8(a0, 0); kf[0][1] = pack8(a0, 1); kf[1][0] = pack8(a1, 0); kf[1][1] = pack8(a1, 1);
      v4u ph[2][4];
      auto kunit = [&](int i) INL {
        v4u phi[4];
        feature_map(kf[i][0], kf[i][1], i ? r1 : r0, i ? r1 : r0, wp, hh, phi);
        f32x16 t0 = {}, t1 = {};
        t0 = mfma(phi[0], id[0], t0); t1 = mfma(phi[2], id[0], t1);
        t0 = mfma(phi[1], id[1], t0); t1 = mfma(phi[3], id[1], t1);
        ph[i][0] = pack8(t0, 0); ph[i][1] = pack8(t0, 1); ph[i][2] = pack8(t1, 0); ph[i][3] = pack8(t1, 1);
        PIN(ph[i][0]); PIN(ph[i][1]); PIN(ph[i][2]); PIN(ph[i][3]);
      };
      kunit(0); kunit(1);
      STAMP(4);
      f32x16 b0 = {}, b1 = {};
      cs = 0.f;
      __builtin_amdgcn_s_setprio(VPRIO);
      lwait4f(M_AL, 19);
#pragma unroll
      for (int i = 0; i < 8; ++i) rd(0, true, i);
      chunk(0, true, b0, b1, 0, 20);
      chunk(1, true, b0, b1, 20, 21); lwait4f(M_AR, 9); v_last();
      chunk(2, true, b0, b1, 21, 22);
      chunk(3, true, b0, b1, 22, 0); waitvm<0>(); lset(al, 24); am = ~0ull;
      chunk(4, true, b0, b1, 24, 0);
      chunk(5, true, b0, b1, 0, 0);
      chunk(6, true, b0, b1, 0, 0);
      STAMP(5);
      w3b_issue();
      cs += other_half(cs);
      f32x16 kv0 = {}, kv1 = {};
      const float c2 = l31 == 28 ? 1.f : -cs;
      const unsigned va = (l31 == 28 ? M_IR : M_MU) + hh * 16;
#pragma unroll
      for (int i = 0; i < 2; ++i) {
        f32x16 v = i ? b1 : b0;
#pragma unroll
        for (int g = 0; g < 4; ++g) {
          const v4f r = ldsf4(L, va + i * 128 + g * 32);
#pragma unroll
          for (int j = 0; j < 4; ++j) v[4 * g + j] = __builtin_fmaf(r[j], c2, v[4 * g + j]);
        }
        const v4u vf0 = pack8(v, 0), vf1 = pack8(v, 1);
        kv0 = mfma(ph[i][0], vf0, kv0); kv1 = mfma(ph[i][2], vf0, kv1);
        kv0 = mfma(ph[i][1], vf1, kv0); kv1 = mfma(ph[i][3], vf1, kv1);
      }
      PIN(kv0); PIN(kv1);
      waitvm<0>(); lset(al, 25);
      v4u u1 = pack8(kv0, 1), u3 = pack8(kv1, 1);
      u1.z = hh ? 0u : u1.z; u1.w = hh ? 0u : u1.w; u3.z = hh ? 0u : u3.z; u3.w = hh ? 0u : u3.w;
      const unsigned so = KVP_OFF + (head * 4 * 32 + rb) * 1024;
      gst(pack8(kv0, 0), rsW, l16, so);
      gst(u1, rsW, l16, so + 32768);
      gst(pack8(kv1, 0), rsW, l16, so + 2 * 32768);
      gst(u3, rsW, l16, so + 3 * 32768);
      __builtin_amdgcn_s_setprio(0);
      publish(ws, FKVP + rb * 16 + head, epoch);
      STAMP(6);
    }
    const rsrc_t rs4 = __builtin_amdgcn_make_buffer_rsrc((void*)w4, 0, 1792 * 896, 0x00020000);
    const rsrc_t rsO = __builtin_amdgcn_make_buffer_rsrc((void*)wo, 0, 448 * 896, 0x00020000);
    auto dma_d2 = [&](int qi) INL {
      dman<2, 8 * 896>(DRING[qi % 12] * SLOT + aw * 2048, vW896, rs4, (64 * (wrap((qi >> 1) + r4, 14) + 14 * (qi & 1)) + 16 * aw) * 896 + 128 * c);
    };
    if (aw < 2) {
      lwait4(M_AL, 25);
      const unsigned wb = aw ? wn[1] : wn[0];
      float cs = 0.f;
      v4u f[2][4];
#pragma unroll
      for (int j = 0; j <= 7; ++j) {
        if (j < 7) {
#pragma unroll
          for (int s = 0; s < 4; ++s) f[j & 1][s] = wfrag(L, wb + j * SLOT + s * 2048, wb + j * SLOT + s * 2048 + 512);
        }
        SCHED();
        if (j > 0) {
#pragma unroll
          for (int s = 0; s < 4; ++s) cs = sum8(f[(j - 1) & 1][s], cs);
          PIN(cs);
        }
        SCHED();
      }
      *(LAS float*)(L + M_CS3 + 256 + aw * 128 + l31 * 4) = cs + other_half(cs);
      lset(M_ACS + 4 * aw, 1);
    }
    STAMP(7);
    lwait4(M_AR, 14); lwait4(M_BR, 10);
#pragma unroll
    for (int qi = 0; qi < 12; ++qi) dma_d2(qi);
    v4u eh[14], dh[32];
#pragma unroll
    for (int p = 0; p < 14; ++p) eh[p] = wld(rsO, vW896, (64 * (p >> 1) + 16 * aw + 8 * (p & 1)) * 896 + 128 * c);
    SCHED();
    waitvm<14>(); lset(al, 27);
#pragma unroll
    for (int p = 0; p < 32; ++p) {
      const int qi = 12 + (p >> 1);
      dh[p] = wld(rs4, vW896, (64 * (wrap((qi >> 1) + r4, 14) + 14 * (qi & 1)) + 16 * aw + 8 * (p & 1)) * 896 + 128 * c);
    }
    SCHED();
    STAMP(8);
    const unsigned wr = l16 + aw * 2048;
    lwait4(M_BR, 11);
#pragma unroll
    for (int p = 0; p < 14; ++p) *(LAS v4u*)(L + wr + (p >> 1) * SLOT + (p & 1) * 1024) = eh[p];
    lset(al, 28);
    STAMP(9);
#pragma unroll
    for (int u = 6; u < 14; ++u) {
      lwait4(M_BR, 12 + (u - 6));
#pragma unroll
      for (int p = 0; p < 4; ++p) { *(LAS v4u*)(L + opq(wr + DRING[(2 * u + (p >> 1)) % 12] * SLOT) + (p & 1) * 1024) = dh[4 * (u - 6) + p]; WPACE(); }
      lset(al, d_al(u));
      if (u == OAT && aw == 0) { poll<4, 0, 2>(ws, flag_at(FO + rb * 4 + (lane & 3)), *(const LAS u64*)(L + M_TAG)); lset(M_OR, 1); STAMP(11); }
    }
    STAMP(10);
    STAMP_OUT();
  }
}

extern "C" void kernel_launch(void* const* d_in, const int* in_sizes, int n_in, void* d_out, int out_size, void* d_ws, size_t ws_size,
                              hipStream_t stream) {
  (void)in_sizes; (void)n_in; (void)out_size; (void)ws_size;
  (void)hipFuncSetAttribute((const void*)fused_kernel, hipFuncAttributeMaxDynamicSharedMemorySize, LDS_BYTES);
  fused_kernel<<<256, 512, LDS_BYTES, stream>>>((const unsigned short*)d_in[0], (const unsigned short*)d_in[1], (const unsigned short*)d_in[2],
                                                (const unsigned short*)d_in[3], (const unsigned short*)d_in[5], (const unsigned short*)d_in[6],
                                                (const unsigned short*)d_in[7], (const unsigned short*)d_in[8], (unsigned short*)d_out,
                                                (unsigned char*)d_ws);
}
```
